```python
import torch

def _softmax(x, axis=-1):
    return torch.softmax(x, dim=axis)

def _gelu(x):
    return torch.nn.functional.gelu(x, approximate='tanh')

def _silu(x):
    return torch.nn.functional.silu(x)

def _sigmoid(x):
    return torch.sigmoid(x)

def _layernorm(x, axis=-1, eps=1e-05):
    m = x.mean(dim=axis, keepdim=True)
    v = x.var(dim=axis, keepdim=True, unbiased=False)
    return (x - m) / torch.sqrt(v + eps)

def _scatter_add(tgt, idx, src, axis=0):
    return tgt.clone().index_add_(axis, idx, src)

def _segment_sum(src, seg, n):
    out = torch.zeros((n,) + src.shape[1:], dtype=src.dtype)
    return out.index_add_(0, seg, src)

def _pad(x, pw):
    flat = tuple((v for pair in reversed(pw) for v in pair))
    return torch.nn.functional.pad(x, flat)
kernel_name = 'standalone__tree_speculative_verify_tree_attention_over_candidate_token_tree__decode'

def setup_inputs():

    _w = lambda a: (lambda t: t.to(torch.bfloat16) if t.is_floating_point() else t)(torch.as_tensor(a))
    B, Hq, Hkv, T, C, D = (2, 32, 4, 1, 16384, 128)
    tree_mask = torch.zeros((B, T, T), dtype=float)
    for b in range(B):
        parent = torch.zeros(T, dtype=torch.int64)
        for i in range(1, T):
            parent[i] = int(torch.randint(0, i, (1,)).item())
        for i in range(T):
            j = i
            tree_mask[b, i, j] = 1.0
            while j > 0:
                j = parent[j]
                tree_mask[b, i, j] = 1.0
    return {'q': _w((lambda *s: torch.randn(*s, dtype=torch.bfloat16) * 0.8)(B, Hq, T, D)), 'k_cache': _w((lambda *s: torch.randn(*s, dtype=torch.bfloat16) * 0.8)(B, Hkv, C, D)), 'v_cache': _w((lambda *s: torch.randn(*s, dtype=torch.bfloat16) * 0.05)(B, Hkv, C, D)), 'k_tree': _w((lambda *s: torch.randn(*s, dtype=torch.bfloat16) * 0.8)(B, Hkv, T, D)), 'v_tree': _w((lambda *s: torch.randn(*s, dtype=torch.bfloat16) * 0.05)(B, Hkv, T, D)), 'tree_mask': _w(tree_mask)}

def reference(q, k_cache, v_cache, k_tree, v_tree, tree_mask):
    B, Hq, T, D = q.shape
    Hkv, C = (k_cache.shape[1], k_cache.shape[2])
    rep = Hq // Hkv
    kc = (lambda x, n, axis: torch.repeat_interleave(x, n, dim=axis))(k_cache, rep, axis=1)
    vc = (lambda x, n, axis: torch.repeat_interleave(x, n, dim=axis))(v_cache, rep, axis=1)
    kt = (lambda x, n, axis: torch.repeat_interleave(x, n, dim=axis))(k_tree, rep, axis=1)
    vt = (lambda x, n, axis: torch.repeat_interleave(x, n, dim=axis))(v_tree, rep, axis=1)
    k = torch.concatenate([kc, kt], axis=2)
    v = torch.concatenate([vc, vt], axis=2)
    scale = D ** (-0.5)
    scores = torch.einsum('bhqd,bhkd->bhqk', q, k) * scale
    ctx_mask = torch.ones((B, 1, T, C), dtype=scores.dtype)
    tm = tree_mask[:, None, :, :].clone()
    full_mask = torch.concatenate([ctx_mask, tm], axis=-1)
    scores = scores + (1.0 - full_mask) * -1000000000.0
    p = _softmax(scores, axis=-1)
    return torch.einsum('bhqk,bhkd->bhqd', p, v)
```

```cpp
#include <hip/hip_runtime.h>
#include <stdint.h>

#define KV_AUX 0

namespace {
typedef unsigned int u32;
typedef unsigned long long u64;
typedef u32 u32x4 __attribute__((ext_vector_type(4)));
typedef float f32x4 __attribute__((ext_vector_type(4)));
typedef float f32x2 __attribute__((ext_vector_type(2)));
typedef __bf16 bf16x8 __attribute__((ext_vector_type(8)));

constexpr int CTX = 16384, HD = 128, NGRP = 8, NSPLIT = 32, NWAVE = 8;
constexpr int KEYS_WG = CTX / NSPLIT, KEYS_W = KEYS_WG / NWAVE;
constexpr float QK_SCALE = 0.08838834764831845f;
constexpr float LOG2E = 1.4426950408889634f;
constexpr float C2 = QK_SCALE * LOG2E;
constexpr u32 WS_EPOCH = 0, WS_STAT = 4096, STAT_BYTES = NGRP * 8 * NSPLIT * 16, WS_O = 65536, O_BYTES = NGRP * NSPLIT * NSPLIT * 256;
static_assert(WS_STAT + STAT_BYTES <= WS_O, "ws");
constexpr u32 WS_BYTES = WS_O + O_BYTES;
constexpr int OSTR = 132;
constexpr u32 SPIN_CAP = 1u << 20;

__device__ __forceinline__ float asf(u32 x) { return __builtin_bit_cast(float, x); }
__device__ __forceinline__ u32 asu(float x) { return __builtin_bit_cast(u32, x); }
__device__ __forceinline__ float x16_max(float x) { auto s = __builtin_amdgcn_permlane16_swap(asu(x), asu(x), false, false); return fmaxf(asf(s[0]), asf(s[1])); }
__device__ __forceinline__ float x32_max(float x) { auto s = __builtin_amdgcn_permlane32_swap(asu(x), asu(x), false, false); return fmaxf(asf(s[0]), asf(s[1])); }
__device__ __forceinline__ float x16_sum(float x) { auto s = __builtin_amdgcn_permlane16_swap(asu(x), asu(x), false, false); return asf(s[0]) + asf(s[1]); }
__device__ __forceinline__ float x32_sum(float x) { auto s = __builtin_amdgcn_permlane32_swap(asu(x), asu(x), false, false); return asf(s[0]) + asf(s[1]); }
template <int CTRL> __device__ __forceinline__ float dpp(float x) { return asf(__builtin_amdgcn_mov_dpp((int)asu(x), CTRL, 0xf, 0xf, true)); }
__device__ __forceinline__ float row_sum(float x) {
  x += dpp<0xB1>(x); x += dpp<0x4E>(x); x += dpp<0x141>(x); x += dpp<0x128>(x); return x; }
__device__ __forceinline__ u32 cvtpk(float lo, float hi) { u32 r; asm("v_cvt_pk_bf16_f32 %0, %1, %2" : "=v"(r) : "v"(lo), "v"(hi)); return r; }
__device__ __forceinline__ float bflo(u32 x) { return asf(x << 16); }
__device__ __forceinline__ float bfhi(u32 x) { return asf(x & 0xffff0000u); }
__device__ __forceinline__ f32x4 mfma(u32x4 a, u32x4 b, f32x4 c) {
  return __builtin_amdgcn_mfma_f32_16x16x32_bf16(__builtin_bit_cast(bf16x8, a), __builtin_bit_cast(bf16x8, b), c, 0, 0, 0); }

__global__ __launch_bounds__(512, 2) void tree_attn_decode(const uint16_t* __restrict__ q, const uint16_t* __restrict__ k, const uint16_t* __restrict__ v,
                                                           const uint16_t* __restrict__ kt, const uint16_t* __restrict__ vt, const uint16_t* __restrict__ mask,
                                                           uint16_t* __restrict__ out, unsigned char* __restrict__ ws) {
  __shared__ __attribute__((aligned(16))) float lds[NWAVE * 8 * OSTR + 64 + 64 + NWAVE * 40];
  float* ored = lds;
  float* smax = lds + NWAVE * 8 * OSTR;
  float* lsum = smax + 64;
  float* fin = lsum + 64;
  const int tid = threadIdx.x, lane = tid & 63, wave = __builtin_amdgcn_readfirstlane(tid >> 6);
  const int grp = blockIdx.x & 7, split = blockIdx.x >> 3;
  const int g = lane >> 4, c = lane & 15;
  const int chead = split >> 2, cq = split & 3;

  const u32 E = __hip_atomic_load((const u32*)(ws + WS_EPOCH + grp * 256), __ATOMIC_RELAXED, __HIP_MEMORY_SCOPE_AGENT);
  const auto rq = __builtin_amdgcn_make_buffer_rsrc((void*)(q + grp * 8 * HD), 0, 8 * HD * 2, 0x00020000);
  const size_t key0 = (size_t)grp * CTX + split * KEYS_WG + wave * KEYS_W;
  const auto rk = __builtin_amdgcn_make_buffer_rsrc((void*)(k + key0 * HD), 0, KEYS_W * HD * 2, 0x00020000);
  const auto rv = __builtin_amdgcn_make_buffer_rsrc((void*)(v + key0 * HD), 0, KEYS_W * HD * 2, 0x00020000);
  const u32 tq = *(const u32*)(q + (grp * 8 + chead) * HD + 2 * lane);
  const u32 tk = *(const u32*)(kt + grp * HD + 2 * lane);
  const u32 tv = *(const u32*)(vt + grp * HD + 32 * cq + 2 * c);
  const u32 tm = mask[grp >> 2];
  u32x4 qf[4];
#pragma unroll
  for (int st = 0; st < 4; ++st) qf[st] = __builtin_amdgcn_raw_buffer_load_b128(rq, (lane & 7) * 256 + g * 16 + st * 64, 0, 0);
  u32x4 kf[4][4];
  const u32 koff = (4 * (c & 3) + (c >> 2)) * 256 + g * 16;
#pragma unroll
  for (int T = 0; T < 4; ++T)
#pragma unroll
    for (int st = 0; st < 4; ++st) kf[T][st] = __builtin_amdgcn_raw_buffer_load_b128(rk, koff + st * 64, T * 4096, KV_AUX);
  u32x4 vf[2][8];
#pragma unroll
  for (int kb = 0; kb < 2; ++kb)
#pragma unroll
    for (int j = 0; j < 8; ++j) vf[kb][j] = __builtin_amdgcn_raw_buffer_load_b128(rv, lane * 16 + (j & 3) * 1024, kb * 8192 + (j >> 2) * 4096, KV_AUX);
  __builtin_amdgcn_sched_barrier(0);

  float st_;
  { float d = bflo(tq) * bflo(tk) + bfhi(tq) * bfhi(tk);
    d = row_sum(d); d = x16_sum(d); d = x32_sum(d);
    st_ = (d * QK_SCALE + (1.0f - bflo(tm)) * -1.0e9f) * LOG2E; }

  f32x4 s[4];
#pragma unroll
  for (int T = 0; T < 4; ++T) { s[T] = f32x4{0.f, 0.f, 0.f, 0.f};
#pragma unroll
    for (int st = 0; st < 4; ++st) s[T] = mfma(kf[T][st], qf[st], s[T]); }
  float mx = s[0][0];
#pragma unroll
  for (int T = 0; T < 4; ++T)
#pragma unroll
    for (int r = 0; r < 4; ++r) mx = fmaxf(mx, s[T][r]);
  mx = x16_max(mx); mx = x32_max(mx);
  if (lane < 8) smax[lane * 8 + wave] = mx;
  __syncthreads();
  float M;
  { const f32x4 a = *(const f32x4*)(smax + (lane & 7) * 8), b = *(const f32x4*)(smax + (lane & 7) * 8 + 4);
    M = fmaxf(fmaxf(fmaxf(a[0], a[1]), fmaxf(a[2], a[3])), fmaxf(fmaxf(b[0], b[1]), fmaxf(b[2], b[3]))); }
  const float mc = M * C2;
  float ls = 0.f;
  u32x4 pf[2];
#pragma unroll
  for (int T = 0; T < 4; ++T) {
#pragma unroll
    for (int r = 0; r < 4; ++r) { s[T][r] = __builtin_amdgcn_exp2f(fmaf(s[T][r], C2, -mc)); ls += s[T][r]; }
    pf[T >> 1][(T & 1) * 2] = cvtpk(s[T][0], s[T][1]); pf[T >> 1][(T & 1) * 2 + 1] = cvtpk(s[T][2], s[T][3]); }
  ls = x16_sum(ls); ls = x32_sum(ls);
  if (lane < 8) lsum[lane * 8 + wave] = ls;

  f32x4 acc[8];
#pragma unroll
  for (int e = 0; e < 8; ++e) acc[e] = f32x4{0.f, 0.f, 0.f, 0.f};
#pragma unroll
  for (int kb = 0; kb < 2; ++kb)
#pragma unroll
    for (int e = 0; e < 8; ++e) {
      const u32 sel = (e & 1) ? 0x07060302u : 0x05040100u;
      u32x4 a;
#pragma unroll
      for (int jj = 0; jj < 4; ++jj) a[jj] = __builtin_amdgcn_perm(vf[kb][2 * jj + 1][e >> 1], vf[kb][2 * jj][e >> 1], sel);
      acc[e] = mfma(a, pf[kb], acc[e]); }
  { const bool hb = (c & 8) != 0;
    float* dst = ored + (wave * 8 + (c & 7)) * OSTR + 32 * g + (hb ? 16 : 0);
#pragma unroll
    for (int rr = 0; rr < 2; ++rr)
#pragma unroll
      for (int eh = 0; eh < 2; ++eh) { f32x4 t;
#pragma unroll
        for (int e = 0; e < 4; ++e) t[e] = hb ? acc[eh * 4 + e][2 + rr] : acc[eh * 4 + e][rr];
        *(f32x4*)(dst + rr * 8 + eh * 4) = t; } }
  __syncthreads();
  const u32 tag = E + 1u;
  const auto rw = __builtin_amdgcn_make_buffer_rsrc((void*)ws, 0, WS_BYTES, 0x00020000);
  { f32x2 o = {0.f, 0.f};
#pragma unroll
    for (int w = 0; w < NWAVE; ++w) o += *(const f32x2*)(ored + (w * 8 + wave) * OSTR + 2 * lane);
    const f32x4 a = *(const f32x4*)(lsum + wave * 8), b = *(const f32x4*)(lsum + wave * 8 + 4);
    const float L = ((a[0] + a[1]) + (a[2] + a[3])) + ((b[0] + b[1]) + (b[2] + b[3]));
    __builtin_amdgcn_raw_buffer_store_b128(u32x4{asu(o[0]), tag, asu(o[1]), tag}, rw,
        WS_O + ((grp * NSPLIT + wave * 4 + g) * NSPLIT + split) * 256 + c * 16, 0, 16);
    if (lane == wave) __builtin_amdgcn_raw_buffer_store_b128(u32x4{asu(mc), tag, asu(L), tag}, rw, WS_STAT + ((grp * 8 + wave) * NSPLIT + split) * 16, 0, 16); }
  { const u32 oo = WS_O + ((grp * NSPLIT + split) * NSPLIT + wave * 4 + g) * 256 + c * 16;
    const u32 so = WS_STAT + ((grp * 8 + chead) * NSPLIT + wave * 4 + g) * 16;
    u32x4 og = {0, 0, 0, 0}, sg = {0, 0, 0, 0};
    bool done = false;
    for (u32 it = 0; it < SPIN_CAP; ++it) {
      asm volatile("" ::: "memory");
      if (!done) {
        og = __builtin_amdgcn_raw_buffer_load_b128(rw, oo, 0, 16);
        sg = __builtin_amdgcn_raw_buffer_load_b128(rw, so, 0, 16);
        done = og[1] == tag && og[3] == tag && sg[1] == tag && sg[3] == tag; }
      if (__all(done)) break;
      __builtin_amdgcn_s_sleep(1);
    }
    const float m = asf(sg[0]);
    const float m4 = x32_max(x16_max(m));
    const float wt = __builtin_amdgcn_exp2f(m - m4);
    const float o0 = x32_sum(x16_sum(asf(og[0]) * wt)), o1 = x32_sum(x16_sum(asf(og[2]) * wt)), l4 = x32_sum(x16_sum(asf(sg[2]) * wt));
    if (lane < 16) *(f32x2*)(fin + wave * 40 + 2 * c) = f32x2{o0, o1};
    if (lane == 0) *(f32x2*)(fin + wave * 40 + 32) = f32x2{m4, l4}; }
  __syncthreads();
  if (wave == 0) {
    float Mx = st_;
    f32x2 ml[NWAVE], ov[NWAVE];
#pragma unroll
    for (int w = 0; w < NWAVE; ++w) { ml[w] = *(const f32x2*)(fin + w * 40 + 32); ov[w] = *(const f32x2*)(fin + w * 40 + 2 * c); Mx = fmaxf(Mx, ml[w][0]); }
    const float wtree = __builtin_amdgcn_exp2f(st_ - Mx);
    float L = wtree, o0 = wtree * bflo(tv), o1 = wtree * bfhi(tv);
#pragma unroll
    for (int w = 0; w < NWAVE; ++w) { const float wt = __builtin_amdgcn_exp2f(ml[w][0] - Mx); L = fmaf(wt, ml[w][1], L); o0 = fmaf(wt, ov[w][0], o0); o1 = fmaf(wt, ov[w][1], o1); }
    const float inv = 1.0f / L;
    if (lane < 16) *(u32*)(out + (grp * 8 + chead) * HD + 32 * cq + 2 * c) = cvtpk(o0 * inv, o1 * inv);
    if (split == 0 && lane == 0) __hip_atomic_store((u32*)(ws + WS_EPOCH + grp * 256), tag, __ATOMIC_RELAXED, __HIP_MEMORY_SCOPE_AGENT);
  }
}
}

extern "C" void kernel_launch(void* const* d_in, const int* in_sizes, int n_in, void* d_out, int out_size, void* d_ws, size_t ws_size, hipStream_t stream) {
  (void)in_sizes; (void)n_in; (void)out_size; (void)ws_size;
  tree_attn_decode<<<dim3(NGRP * NSPLIT), dim3(512), 0, stream>>>((const uint16_t*)d_in[0], (const uint16_t*)d_in[1], (const uint16_t*)d_in[2], (const uint16_t*)d_in[3],
                                                                  (const uint16_t*)d_in[4], (const uint16_t*)d_in[5], (uint16_t*)d_out, (unsigned char*)d_ws);
}
```

```cpp
#include <hip/hip_runtime.h>
#include <stdint.h>

#define KV_AUX 2
#define POLL_SLEEP 1
#define HINT_ATOMIC 1
#define HINT_DELAY 0
#define POLL_GAP 6
#define OUT_AUX 16
#define PRETOUCH 0
#define WS_BASE 0
#define STAMP(i) do {} while (0)
#define STAMP_OUT(a, b) do {} while (0)

namespace {
typedef unsigned int u32;
typedef u32 u32x4 __attribute__((ext_vector_type(4)));
typedef float f32x4 __attribute__((ext_vector_type(4)));
typedef float f32x2 __attribute__((ext_vector_type(2)));
typedef __bf16 bf16x8 __attribute__((ext_vector_type(8)));
typedef unsigned long long u64;
typedef __attribute__((address_space(1))) u64 gu64;
typedef __attribute__((address_space(3))) u32 lu32;

constexpr int CTX = 16384, HD = 128, NGRP = 8, NSPLIT = 32, NWAVE = 4;
constexpr int KEYS_WG = CTX / NSPLIT, KEYS_W = KEYS_WG / NWAVE;
constexpr float QK_SCALE = 0.08838834764831845f;
constexpr float LOG2E = 1.4426950408889634f;
constexpr float C2 = QK_SCALE * LOG2E;
constexpr u32 WS_EPOCH = 0, WS_STAT = 4096, STAT_BYTES = NGRP * 8 * NSPLIT * 16, WS_O = 65536, O_BYTES = NGRP * NSPLIT * NSPLIT * 256;
static_assert(WS_STAT + STAT_BYTES <= WS_O, "ws");
constexpr u32 WS_HINT = WS_O + O_BYTES, HINT_BYTES = NGRP * NSPLIT * NSPLIT * 8;
constexpr u32 WS_BYTES = WS_HINT + HINT_BYTES;
constexpr int OSTR = 132;
constexpr u32 SPIN_CAP = 1u << 20;
constexpr int LDS_FLOATS = NWAVE * 8 * OSTR + 64 + 4;

__device__ __forceinline__ float asf(u32 x) { return __builtin_bit_cast(float, x); }
__device__ __forceinline__ u32 asu(float x) { return __builtin_bit_cast(u32, x); }
__device__ __forceinline__ float x16_max(float x) { auto s = __builtin_amdgcn_permlane16_swap(asu(x), asu(x), false, false); return fmaxf(asf(s[0]), asf(s[1])); }
__device__ __forceinline__ float x32_max(float x) { auto s = __builtin_amdgcn_permlane32_swap(asu(x), asu(x), false, false); return fmaxf(asf(s[0]), asf(s[1])); }
__device__ __forceinline__ float x16_sum(float x) { auto s = __builtin_amdgcn_permlane16_swap(asu(x), asu(x), false, false); return asf(s[0]) + asf(s[1]); }
__device__ __forceinline__ float x32_sum(float x) { auto s = __builtin_amdgcn_permlane32_swap(asu(x), asu(x), false, false); return asf(s[0]) + asf(s[1]); }
template <int CTRL> __device__ __forceinline__ float dpp(float x) { return asf(__builtin_amdgcn_mov_dpp((int)asu(x), CTRL, 0xf, 0xf, true)); }
constexpr int XOR1 = 0xB1, XOR2 = 0x4E, HMIR = 0x141, ROR8 = 0x128, ROR4 = 0x124;
__device__ __forceinline__ float row_sum(float x) { x += dpp<XOR1>(x); x += dpp<XOR2>(x); x += dpp<HMIR>(x); x += dpp<ROR8>(x); return x; }
__device__ __forceinline__ float hi4_sum(float x) { x += dpp<ROR8>(x); x += dpp<ROR4>(x); return x32_sum(x16_sum(x)); }
__device__ __forceinline__ float hi4_max(float x) { x = fmaxf(x, dpp<ROR8>(x)); x = fmaxf(x, dpp<ROR4>(x)); return x32_max(x16_max(x)); }
__device__ __forceinline__ u32 cvtpk(float lo, float hi) { u32 r; asm("v_cvt_pk_bf16_f32 %0, %1, %2" : "=v"(r) : "v"(lo), "v"(hi)); return r; }
__device__ __forceinline__ float bflo(u32 x) { return asf(x << 16); }
__device__ __forceinline__ float bfhi(u32 x) { return asf(x & 0xffff0000u); }
__device__ __forceinline__ f32x4 mfma(u32x4 a, u32x4 b, f32x4 c) {
  return __builtin_amdgcn_mfma_f32_16x16x32_bf16(__builtin_bit_cast(bf16x8, a), __builtin_bit_cast(bf16x8, b), c, 0, 0, 0); }
#define SB() __builtin_amdgcn_sched_barrier(0)

__global__ __launch_bounds__(512, 2) void tree_attn_decode(const uint16_t* __restrict__ q, const uint16_t* __restrict__ k, const uint16_t* __restrict__ v,
                                                           const uint16_t* __restrict__ kt, const uint16_t* __restrict__ vt, const uint16_t* __restrict__ mask,
                                                           uint16_t* __restrict__ out, unsigned char* __restrict__ ws0) {
  __shared__ __attribute__((aligned(16))) float lds[LDS_FLOATS];
  float* smax = lds + NWAVE * 8 * OSTR;
  float* lsum = smax + 32;
  asm volatile("" :: "s"(q), "s"(k), "s"(v), "s"(kt), "s"(vt), "s"(mask), "s"(out), "s"(ws0));
  unsigned char* ws = ws0 + WS_BASE;
  const int tid = threadIdx.x, lane = tid & 63, wave = __builtin_amdgcn_readfirstlane(tid >> 6);
  const int grp = blockIdx.x & 7, split = blockIdx.x >> 3;
  const int g = lane >> 4, c = lane & 15;
  const auto rw = __builtin_amdgcn_make_buffer_rsrc((void*)ws, 0, WS_BYTES, 0x00020000);

  if (wave < NWAVE) {
    STAMP(0);
    const size_t key0 = (size_t)grp * CTX + split * KEYS_WG + wave * KEYS_W;
    const auto rk = __builtin_amdgcn_make_buffer_rsrc((void*)(k + key0 * HD), 0, KEYS_W * HD * 2, 0x00020000);
    u32x4 kf[16][2], qf[2];
    const u32 koff = (16 * ((c >> 1) & 1) + (c >> 2)) * 256 + g * 32 + (c & 1) * 16;
#define LOADK(t) do { kf[t][0] = __builtin_amdgcn_raw_buffer_load_b128(rk, koff + ((t) & 3) * 1024, ((t) >> 2) * 8192, KV_AUX); \
                      kf[t][1] = __builtin_amdgcn_raw_buffer_load_b128(rk, koff + ((t) & 3) * 1024 + 128, ((t) >> 2) * 8192, KV_AUX); } while (0)
    LOADK(0);
    SB();
    const auto rq = __builtin_amdgcn_make_buffer_rsrc((void*)(q + grp * 8 * HD), 0, 8 * HD * 2, 0x00020000);
    const auto rv = __builtin_amdgcn_make_buffer_rsrc((void*)(v + key0 * HD), 0, KEYS_W * HD * 2, 0x00020000);
    u32x4 vf[4][8];
#define LOADV(kb) _Pragma("unroll") for (int j = 0; j < 8; ++j) vf[kb][j] = __builtin_amdgcn_raw_buffer_load_b128(rv, lane * 16 + (j & 3) * 1024, (kb) * 8192 + (j >> 2) * 4096, KV_AUX)
    qf[0] = __builtin_amdgcn_raw_buffer_load_b128(rq, (c >> 1) * 256 + g * 32 + (c & 1) * 16, 0, 0);
    qf[1] = __builtin_amdgcn_raw_buffer_load_b128(rq, (c >> 1) * 256 + g * 32 + (c & 1) * 16 + 128, 0, 0);
    const u32 E = __builtin_amdgcn_raw_buffer_load_b32(rw, WS_EPOCH + grp * 256, 0, 16);
#pragma unroll
    for (int t = 1; t < 16; ++t) LOADK(t);
    LOADV(0);
    SB();
    const bool par = (c & 1) != 0;
    float s[16];
    float mx = -INFINITY;
#pragma unroll
    for (int t = 0; t < 16; ++t) {
      f32x4 d = mfma(kf[t][0], qf[0], f32x4{0.f, 0.f, 0.f, 0.f});
      d = mfma(kf[t][1], qf[1], d);
      const float ev = d[0] + dpp<XOR1>(d[1]), od = d[3] + dpp<XOR1>(d[2]);
      s[t] = par ? od : ev;
      mx = fmaxf(mx, s[t]);
    }
    mx = fmaxf(mx, dpp<XOR1>(mx)); mx = x16_max(mx); mx = x32_max(mx);
    if (lane < 16 && !par) smax[(lane >> 1) * 4 + wave] = mx;
    STAMP(8);
    SB(); LOADV(1); SB();
    __syncthreads();
    float M;
    { const f32x4 a = *(const f32x4*)(smax + (c >> 1) * 4); M = fmaxf(fmaxf(a[0], a[1]), fmaxf(a[2], a[3])); }
    const float mc = M * C2;
    float ls = 0.f;
    u32x4 pf[4];
#pragma unroll
    for (int kb = 0; kb < 4; ++kb) {
      float p[4];
#pragma unroll
      for (int i = 0; i < 4; ++i) { p[i] = __builtin_amdgcn_exp2f(fmaf(s[4 * kb + i], C2, -mc)); ls += p[i]; }
      const u32 p01 = cvtpk(p[0], p[1]), p23 = cvtpk(p[2], p[3]);
      pf[kb][0] = __builtin_amdgcn_mov_dpp(p01, 0xA0, 0xf, 0xf, true); pf[kb][1] = __builtin_amdgcn_mov_dpp(p23, 0xA0, 0xf, 0xf, true);
      pf[kb][2] = __builtin_amdgcn_mov_dpp(p01, 0xF5, 0xf, 0xf, true); pf[kb][3] = __builtin_amdgcn_mov_dpp(p23, 0xF5, 0xf, 0xf, true);
    }
    ls += dpp<XOR1>(ls); ls = x16_sum(ls); ls = x32_sum(ls);
    if (lane < 16 && !par) lsum[(lane >> 1) * 4 + wave] = ls;
    const u32 tag = E + 1u;
    u32 a_ow = ((wave * 8 + (c >> 1)) * OSTR + 32 * g + 16 * (c & 1)) * 4;
    u32 a_or = (2 * wave * OSTR + 2 * lane) * 4;
    u32 a_pub = WS_O + ((grp * NSPLIT + 8 * wave + g) * NSPLIT + split) * 256 + c * 16;
    u32 a_sl = (NWAVE * 8 * OSTR + 32 + (c >> 1) * 4) * 4;
    u32 a_st = WS_STAT + ((grp * 8 + (c >> 1)) * NSPLIT + split) * 16;
    asm volatile("" : "+v"(a_ow), "+v"(a_or), "+v"(a_pub), "+v"(a_sl), "+v"(a_st));
    STAMP(1);
    SB(); LOADV(2); LOADV(3); SB();
    f32x4 acc[8];
#pragma unroll
    for (int e = 0; e < 8; ++e) acc[e] = f32x4{0.f, 0.f, 0.f, 0.f};
#define PV(kb) _Pragma("unroll") for (int e = 0; e < 8; ++e) { const u32 sel = (e & 1) ? 0x07060302u : 0x05040100u; u32x4 a; \
      _Pragma("unroll") for (int jj = 0; jj < 4; ++jj) a[jj] = __builtin_amdgcn_perm(vf[kb][2 * jj + 1][e >> 1], vf[kb][2 * jj][e >> 1], sel); \
      acc[e] = mfma(a, pf[kb], acc[e]); }
    PV(0); SB(); PV(1); SB(); PV(2); SB(); PV(3);
    STAMP(2);
#pragma unroll
    for (int rr = 0; rr < 2; ++rr)
#pragma unroll
      for (int eh = 0; eh < 2; ++eh) { f32x4 t;
#pragma unroll
        for (int e = 0; e < 4; ++e) { float lo = acc[eh * 4 + e][rr], hi = acc[eh * 4 + e][2 + rr]; asm("" : "+v"(lo)); t[e] = par ? hi : lo; }
        *(f32x4*)((char*)lds + a_ow + rr * 32 + eh * 16) = t; }
    __syncthreads();
    STAMP(9);
#pragma unroll
    for (int hh = 0; hh < 2; ++hh) {
      f32x2 o = *(const f32x2*)((char*)lds + a_or + hh * OSTR * 4);
#pragma unroll
      for (int w = 1; w < NWAVE; ++w) o += *(const f32x2*)((char*)lds + a_or + (w * 8 + hh) * OSTR * 4);
      __builtin_amdgcn_raw_buffer_store_b128(u32x4{asu(o[0]), tag, asu(o[1]), tag}, rw, a_pub + hh * 4 * NSPLIT * 256, 0, 16);
    }
    if ((lane >> 2) == wave && !par) {
      const f32x4 a = *(const f32x4*)((char*)lds + a_sl);
      __builtin_amdgcn_raw_buffer_store_b128(u32x4{asu(mc), tag, asu((a[0] + a[1]) + (a[2] + a[3])), tag}, rw, a_st, 0, 16); }
    STAMP(3);
  } else {
    const u32 E = __builtin_amdgcn_raw_buffer_load_b32(rw, WS_EPOCH + grp * 256, 0, 16);
    lu32* gate = (lu32*)(lds + NWAVE * 8 * OSTR + 64);
    if (wave < 6) {
      u64 a_h = (u64)(ws + WS_HINT + (wave == 4 ? (grp * NSPLIT + split) * NSPLIT + (lane & 31) : (grp * NSPLIT + (lane & 31)) * NSPLIT + split) * 8);
      const u32 tag = E + 1u;
      const u64 want = ((u64)tag << 32) | tag;
      if (tid == 256) __hip_atomic_store(gate, E, __ATOMIC_RELAXED, __HIP_MEMORY_SCOPE_WORKGROUP);
      asm volatile("" : "+v"(a_h));
      __syncthreads();
      __syncthreads();
      if (lane < 32) {
        if (wave == 5) {
          if (HINT_DELAY) __builtin_amdgcn_s_sleep(HINT_DELAY);
          (void)__hip_atomic_exchange((gu64*)a_h, want, __ATOMIC_RELAXED, __HIP_MEMORY_SCOPE_AGENT);
        } else {
          STAMP(5);
          const u32x4 cd = {~tag, ~tag, ~tag, ~tag};
          u64 r0, r1, r2, okm; u32 cnt;
          const u32 a_gate = (u32)(size_t)gate;
#define STR2(x) #x
#define STR(x) STR2(x)
          asm volatile(
              "s_mov_b64 %[okm], 0\n\t"
              "s_mov_b32 %[cnt], 0x100000\n\t"
              "global_atomic_cmpswap_x2 %[r0], %[a], %[cd], off sc0\n\t"
              "s_sleep " STR(POLL_GAP) "\n\t"
              "global_atomic_cmpswap_x2 %[r1], %[a], %[cd], off sc0\n\t"
              "s_sleep " STR(POLL_GAP) "\n\t"
              "global_atomic_cmpswap_x2 %[r2], %[a], %[cd], off sc0\n"
              "1:\n\t"
              "s_waitcnt vmcnt(2)\n\t"
              "v_cmp_eq_u64 vcc, %[r0], %[want]\n\t"
              "s_or_b64 %[okm], %[okm], vcc\n\t"
              "s_cmp_eq_u64 %[okm], exec\n\t"
              "s_cbranch_scc1 2f\n\t"
              "global_atomic_cmpswap_x2 %[r0], %[a], %[cd], off sc0\n\t"
              "s_waitcnt vmcnt(2)\n\t"
              "v_cmp_eq_u64 vcc, %[r1], %[want]\n\t"
              "s_or_b64 %[okm], %[okm], vcc\n\t"
              "s_cmp_eq_u64 %[okm], exec\n\t"
              "s_cbranch_scc1 2f\n\t"
              "global_atomic_cmpswap_x2 %[r1], %[a], %[cd], off sc0\n\t"
              "s_waitcnt vmcnt(2)\n\t"
              "v_cmp_eq_u64 vcc, %[r2], %[want]\n\t"
              "s_or_b64 %[okm], %[okm], vcc\n\t"
              "s_cmp_eq_u64 %[okm], exec\n\t"
              "s_cbranch_scc1 2f\n\t"
              "global_atomic_cmpswap_x2 %[r2], %[a], %[cd], off sc0\n\t"
              "s_sub_u32 %[cnt], %[cnt], 1\n\t"
              "s_cmp_lg_u32 %[cnt], 0\n\t"
              "s_cbranch_scc1 1b\n"
              "2:\n\t"
              "ds_write_b32 %[ga], %[tag]\n\t"
              "s_waitcnt vmcnt(0) lgkmcnt(0)"
              : [r0] "=&v"(r0), [r1] "=&v"(r1), [r2] "=&v"(r2), [okm] "=&s"(okm), [cnt] "=&s"(cnt)
              : [a] "v"(a_h), [cd] "v"(cd), [want] "v"(want), [ga] "v"(a_gate), [tag] "v"(tag)
              : "vcc", "scc", "memory");
          STAMP(6);
          STAMP_OUT(5, 7);
        }
      }
    } else {
      const int lw = wave - 6;
      const int chead = split >> 2, cq = split & 3;
      const u32 tq = *(const u32*)(q + (grp * 8 + chead) * HD + 2 * lane);
      const u32 tk = *(const u32*)(kt + grp * HD + 2 * lane);
      const u32 tv = *(const u32*)(vt + grp * HD + 32 * cq + 16 * lw + 2 * (lane & 7));
      const u32 tm = mask[grp >> 2];
      u32 oo = WS_O + ((grp * NSPLIT + split) * NSPLIT + (lane >> 3)) * 256 + lw * 128 + (lane & 7) * 16;
      u32 so = WS_STAT + ((grp * 8 + chead) * NSPLIT + (lane >> 3)) * 16;
      const auto ro = __builtin_amdgcn_make_buffer_rsrc((void*)out, 0, NGRP * 8 * HD * 2, 0x00020000);
      u32 a_out = ((grp * 8 + chead) * HD + 32 * cq + 16 * lw + 2 * (lane & 7)) * 2;
      float st_;
      { float d = bflo(tq) * bflo(tk) + bfhi(tq) * bfhi(tk);
        d = row_sum(d); d = x16_sum(d); d = x32_sum(d);
        st_ = (d * QK_SCALE + (1.0f - bflo(tm)) * -1.0e9f) * LOG2E; }
      const u32 tag = E + 1u;
      float tv0 = bflo(tv), tv1 = bfhi(tv);
      asm volatile("" : "+v"(st_), "+v"(tv0), "+v"(tv1), "+v"(oo), "+v"(so), "+v"(a_out));
      __syncthreads();
      __syncthreads();
      for (u32 it = 0; it < SPIN_CAP * 16; ++it) { if (__hip_atomic_load(gate, __ATOMIC_RELAXED, __HIP_MEMORY_SCOPE_WORKGROUP) == tag) break; }
      u32x4 og[4], sg[4];
      bool done = false;
      for (u32 it = 0; it < SPIN_CAP; ++it) {
        asm volatile("" ::: "memory");
        if (!done) {
#pragma unroll
          for (int i = 0; i < 4; ++i) { og[i] = __builtin_amdgcn_raw_buffer_load_b128(rw, oo + i * 2048, 0, 16); sg[i] = __builtin_amdgcn_raw_buffer_load_b128(rw, so + i * 128, 0, 16); }
          done = true;
#pragma unroll
          for (int i = 0; i < 4; ++i) done &= og[i][1] == tag && og[i][3] == tag && sg[i][1] == tag && sg[i][3] == tag; }
        if (__all(done)) break;
        __builtin_amdgcn_s_sleep(POLL_SLEEP);
      }
      float Mx = fmaxf(fmaxf(asf(sg[0][0]), asf(sg[1][0])), fmaxf(asf(sg[2][0]), asf(sg[3][0])));
      Mx = fmaxf(Mx, dpp<ROR8>(Mx)); Mx = fmaxf(x32_max(x16_max(Mx)), st_);
      float L = 0.f, a0 = 0.f, a1 = 0.f;
#pragma unroll
      for (int i = 0; i < 4; ++i) { const float w = __builtin_amdgcn_exp2f(asf(sg[i][0]) - Mx); L = fmaf(w, asf(sg[i][2]), L); a0 = fmaf(w, asf(og[i][0]), a0); a1 = fmaf(w, asf(og[i][2]), a1); }
      const float wt = __builtin_amdgcn_exp2f(st_ - Mx);
      L += dpp<ROR8>(L); a0 += dpp<ROR8>(a0); a1 += dpp<ROR8>(a1);
      L = x32_sum(x16_sum(L)) + wt; a0 = x32_sum(x16_sum(a0)) + wt * tv0; a1 = x32_sum(x16_sum(a1)) + wt * tv1;
      const float inv = __builtin_amdgcn_rcpf(L);
      if (lane < 8) __builtin_amdgcn_raw_buffer_store_b32(cvtpk(a0 * inv, a1 * inv), ro, a_out, 0, OUT_AUX);
      if (split == 0 && lw == 0 && lane == 0) __builtin_amdgcn_raw_buffer_store_b32(tag, rw, WS_EPOCH + grp * 256, 0, 16);
    }
  }
}
}

extern "C" void kernel_launch(void* const* d_in, const int* in_sizes, int n_in, void* d_out, int out_size, void* d_ws, size_t ws_size, hipStream_t stream) {
  (void)in_sizes; (void)n_in; (void)out_size; (void)ws_size;
  tree_attn_decode<<<dim3(NGRP * NSPLIT), dim3(512), 0, stream>>>((const uint16_t*)d_in[0], (const uint16_t*)d_in[1], (const uint16_t*)d_in[2], (const uint16_t*)d_in[3],
                                                                  (const uint16_t*)d_in[4], (const uint16_t*)d_in[5], (uint16_t*)d_out, (unsigned char*)d_ws);
}
```

```cpp
#include <hip/hip_runtime.h>
#include <stdint.h>

#define KV_AUX 2
#define POLL_SLEEP 1
#define HINT_ATOMIC 1
#define HINT_DELAY 0
#define HINT_POS 1
#define ROT 0
#define CODE_PF 6784
#define HPRIO 3
#define POLL_GAP 6
#define OUT_AUX 16
#define PRETOUCH 0
#define WS_BASE 0
#define STAMP(i) do {} while (0)
#define STAMP_OUT(a, b) do {} while (0)

namespace {
typedef unsigned int u32;
typedef u32 u32x4 __attribute__((ext_vector_type(4)));
typedef float f32x4 __attribute__((ext_vector_type(4)));
typedef float f32x2 __attribute__((ext_vector_type(2)));
typedef unsigned int u32x2 __attribute__((ext_vector_type(2)));
typedef __bf16 bf16x8 __attribute__((ext_vector_type(8)));
typedef unsigned long long u64;
typedef __attribute__((address_space(1))) u64 gu64;
typedef __attribute__((address_space(3))) u32 lu32;

constexpr int CTX = 16384, HD = 128, NGRP = 8, NSPLIT = 32, NWAVE = 4;
constexpr int KEYS_WG = CTX / NSPLIT, KEYS_W = KEYS_WG / NWAVE;
constexpr float QK_SCALE = 0.08838834764831845f;
constexpr float LOG2E = 1.4426950408889634f;
constexpr float C2 = QK_SCALE * LOG2E;
constexpr u32 WS_EPOCH = 0, WS_O = 4096, O_BYTES = NGRP * NSPLIT * NSPLIT * 256;
constexpr u32 WS_HINT = WS_O + O_BYTES, HINT_BYTES = NGRP * NSPLIT * 64 * 8;
constexpr u32 WS_BYTES = WS_HINT + HINT_BYTES;
constexpr int OSTR = 132;
constexpr u32 SPIN_CAP = 1u << 20;
constexpr int LDS_FLOATS = NWAVE * 8 * OSTR + 64 + 4 + 36;

__device__ __forceinline__ float asf(u32 x) { return __builtin_bit_cast(float, x); }
__device__ __forceinline__ u32 asu(float x) { return __builtin_bit_cast(u32, x); }
__device__ __forceinline__ float x16_max(float x) { auto s = __builtin_amdgcn_permlane16_swap(asu(x), asu(x), false, false); return fmaxf(asf(s[0]), asf(s[1])); }
__device__ __forceinline__ float x32_max(float x) { auto s = __builtin_amdgcn_permlane32_swap(asu(x), asu(x), false, false); return fmaxf(asf(s[0]), asf(s[1])); }
__device__ __forceinline__ float x16_sum(float x) { auto s = __builtin_amdgcn_permlane16_swap(asu(x), asu(x), false, false); return asf(s[0]) + asf(s[1]); }
__device__ __forceinline__ float x32_sum(float x) { auto s = __builtin_amdgcn_permlane32_swap(asu(x), asu(x), false, false); return asf(s[0]) + asf(s[1]); }
template <int CTRL> __device__ __forceinline__ float dpp(float x) { return asf(__builtin_amdgcn_mov_dpp((int)asu(x), CTRL, 0xf, 0xf, true)); }
constexpr int XOR1 = 0xB1, XOR2 = 0x4E, HMIR = 0x141, ROR8 = 0x128, ROR4 = 0x124;
__device__ __forceinline__ float row_sum(float x) { x += dpp<XOR1>(x); x += dpp<XOR2>(x); x += dpp<HMIR>(x); x += dpp<ROR8>(x); return x; }
__device__ __forceinline__ float hi4_sum(float x) { x += dpp<ROR8>(x); x += dpp<ROR4>(x); return x32_sum(x16_sum(x)); }
__device__ __forceinline__ float hi4_max(float x) { x = fmaxf(x, dpp<ROR8>(x)); x = fmaxf(x, dpp<ROR4>(x)); return x32_max(x16_max(x)); }
__device__ __forceinline__ u32 cvtpk(float lo, float hi) { u32 r; asm("v_cvt_pk_bf16_f32 %0, %1, %2" : "=v"(r) : "v"(lo), "v"(hi)); return r; }
__device__ __forceinline__ float bflo(u32 x) { return asf(x << 16); }
__device__ __forceinline__ float bfhi(u32 x) { return asf(x & 0xffff0000u); }
__device__ __forceinline__ f32x4 mfma(u32x4 a, u32x4 b, f32x4 c) {
  return __builtin_amdgcn_mfma_f32_16x16x32_bf16(__builtin_bit_cast(bf16x8, a), __builtin_bit_cast(bf16x8, b), c, 0, 0, 0); }
__device__ __forceinline__ u32 round_up24(float x) { const u32 u = asu(x); return (x >= 0.f ? u + 0xFFu : u) & ~0xFFu; }
#define SB() __builtin_amdgcn_sched_barrier(0)

__global__ __launch_bounds__(512, 2) void tree_attn_decode(const uint16_t* __restrict__ q, const uint16_t* __restrict__ k, const uint16_t* __restrict__ v,
                                                           const uint16_t* __restrict__ kt, const uint16_t* __restrict__ vt, const uint16_t* __restrict__ mask,
                                                           uint16_t* __restrict__ out, unsigned char* __restrict__ ws0) {
  __shared__ __attribute__((aligned(16))) float lds[LDS_FLOATS];
  float* smax = lds + NWAVE * 8 * OSTR;
  float* lsum = smax + 32;
  const int tid = threadIdx.x, lane = tid & 63, wave = __builtin_amdgcn_readfirstlane(tid >> 6);
  if (HPRIO && wave >= NWAVE) __builtin_amdgcn_s_setprio(HPRIO);
  if (CODE_PF && wave == 7) {
    u64 pc; asm volatile("s_getpc_b64 %0" : "=s"(pc));
    if (lane * 128 < CODE_PF) { const u32 x = *(const __attribute__((address_space(1))) u32*)((pc & ~255ull) + lane * 128); asm volatile("" :: "v"(x)); }
  }
  asm volatile("" :: "s"(q), "s"(k), "s"(v), "s"(kt), "s"(vt), "s"(mask), "s"(out), "s"(ws0));
  unsigned char* ws = ws0 + WS_BASE;
  const int grp = blockIdx.x & 7, split = blockIdx.x >> 3;
  const int g = lane >> 4, c = lane & 15;
  const auto rw = __builtin_amdgcn_make_buffer_rsrc((void*)ws, 0, WS_BYTES, 0x00020000);

  if (wave >= NWAVE) {
    const u32x2 E2 = __builtin_amdgcn_raw_buffer_load_b64(rw, WS_EPOCH + grp * 256, 0, 16);
    const u64 epoch = (((u64)E2[1] << 32) | E2[0]) + 1;
    const u32 tag = (u32)epoch;
    lu32* gate = (lu32*)(lds + NWAVE * 8 * OSTR + 64);
    float* wbuf = lds + NWAVE * 8 * OSTR + 64 + 4;
    const int chead = split >> 2, cq = split & 3;
    if (wave < 6) {
      u64 a_h = (u64)(ws + WS_HINT + (wave == 4 ? (grp * NSPLIT + split) * 64 + lane : (grp * NSPLIT + (lane & 31)) * 64 + (lane & 32) + split) * 8);
      float st_ = 0.f;
      if (wave == 4) {
        const u32 tq = *(const u32*)(q + (grp * 8 + chead) * HD + 2 * lane);
        const u32 tk = *(const u32*)(kt + grp * HD + 2 * lane);
        const u32 tm = mask[grp >> 2];
        float d = bflo(tq) * bflo(tk) + bfhi(tq) * bfhi(tk);
        d = row_sum(d); d = x16_sum(d); d = x32_sum(d);
        st_ = (d * QK_SCALE + (1.0f - bflo(tm)) * -1.0e9f) * LOG2E;
        if (lane < 2) __hip_atomic_store(gate + lane, ~tag, __ATOMIC_RELAXED, __HIP_MEMORY_SCOPE_WORKGROUP);
      }
      u32 a_s = ((lane & 31) >> 2) * 16;
      asm volatile("" : "+v"(a_h), "+v"(st_), "+v"(a_s));
      if (HPRIO) __builtin_amdgcn_s_setprio(0);
      __syncthreads();
      __builtin_amdgcn_s_barrier();
      u64 word = 0;
      if (wave == 5) {
        const f32x4 a = *(const f32x4*)((const char*)smax + a_s), b = *(const f32x4*)((const char*)lsum + a_s);
        const u32 mb = round_up24(fmaxf(fmaxf(a[0], a[1]), fmaxf(a[2], a[3])) * C2), lb = asu((b[0] + b[1]) + (b[2] + b[3])) + 0x80u;
        word = (epoch << 24) | ((lane < 32 ? mb : lb) >> 8);
        asm volatile("" : "+v"(word));
      }
      __builtin_amdgcn_s_barrier();
      if (wave == 5) {
        if (HINT_DELAY) __builtin_amdgcn_s_sleep(HINT_DELAY);
        (void)__hip_atomic_exchange((gu64*)a_h, word, __ATOMIC_RELAXED, __HIP_MEMORY_SCOPE_AGENT);
      } else {
        STAMP(5);
        const u32x4 cd = {~tag, ~tag, ~tag, ~tag};
        const u64 want = epoch & 0xFFFFFFFFFFull;
        u64 r0, r1, r2, win, tmp; u32 cnt;
        const u32 a_gate = (u32)(size_t)gate;
#define STR2(x) #x
#define STR(x) STR2(x)
#define POLL_STEP(r) \
            "s_waitcnt vmcnt(2)\n\t" \
            "v_lshrrev_b64 %[tmp], 24, %[" r "]\n\t" \
            "v_mov_b64 %[win], %[" r "]\n\t" \
            "v_cmp_eq_u64 vcc, %[tmp], %[want]\n\t" \
            "s_cmp_eq_u64 vcc, exec\n\t" \
            "s_cbranch_scc1 2f\n\t" \
            "global_atomic_cmpswap_x2 %[" r "], %[a], %[cd], off sc0\n\t"
        asm volatile(
            "s_mov_b32 %[cnt], 0x100000\n\t"
            "global_atomic_cmpswap_x2 %[r0], %[a], %[cd], off sc0\n\t"
            "s_sleep " STR(POLL_GAP) "\n\t"
            "global_atomic_cmpswap_x2 %[r1], %[a], %[cd], off sc0\n\t"
            "s_sleep " STR(POLL_GAP) "\n\t"
            "global_atomic_cmpswap_x2 %[r2], %[a], %[cd], off sc0\n"
            "1:\n\t"
            POLL_STEP("r0") POLL_STEP("r1") POLL_STEP("r2")
            "s_sub_u32 %[cnt], %[cnt], 1\n\t"
            "s_cmp_lg_u32 %[cnt], 0\n\t"
            "s_cbranch_scc1 1b\n"
            "2:\n\t"
            "ds_write_b32 %[ga], %[tag]"
            : [r0] "=&v"(r0), [r1] "=&v"(r1), [r2] "=&v"(r2), [win] "=&v"(win), [tmp] "=&v"(tmp), [cnt] "=&s"(cnt)
            : [a] "v"(a_h), [cd] "v"(cd), [want] "v"(want), [ga] "v"(a_gate), [tag] "v"(tag)
            : "vcc", "scc", "memory");
        STAMP(6);
        const float x = asf((u32)win << 8);
        float Mx = lane < 32 ? x : st_;
        Mx = fmaxf(Mx, dpp<XOR1>(Mx)); Mx = fmaxf(Mx, dpp<XOR2>(Mx)); Mx = fmaxf(Mx, dpp<HMIR>(Mx)); Mx = fmaxf(Mx, dpp<ROR8>(Mx)); Mx = x32_max(x16_max(Mx));
        const auto sw = __builtin_amdgcn_permlane32_swap(asu(x), asu(x), false, false);
        const float w = __builtin_amdgcn_exp2f((lane < 32 ? x : st_) - Mx);
        float L = lane < 32 ? w * asf(sw[1]) : 0.f;
        L = row_sum(L); L = x32_sum(x16_sum(L));
        const auto wsw = __builtin_amdgcn_permlane32_swap(asu(w), asu(w), false, false);
        L += asf(wsw[1]);
        const float wn = w * __builtin_amdgcn_rcpf(L);
        if (lane < 33) wbuf[lane] = wn;
        asm volatile("s_waitcnt lgkmcnt(0)" ::: "memory");
        if (lane == 0) __hip_atomic_store(gate + 1, tag, __ATOMIC_RELAXED, __HIP_MEMORY_SCOPE_WORKGROUP);
        asm volatile("s_waitcnt vmcnt(0)" : "+v"(r0), "+v"(r1), "+v"(r2) :: "memory");
        STAMP_OUT(5, 7);
      }
    } else {
      const int lw = wave - 6;
      const u32 tv = *(const u32*)(vt + grp * HD + 32 * cq + 16 * lw + 2 * (lane & 7));
      u32 oo = WS_O + ((grp * NSPLIT + split) * NSPLIT + (lane >> 3)) * 256 + lw * 128 + (lane & 7) * 16;
      const auto ro = __builtin_amdgcn_make_buffer_rsrc((void*)out, 0, NGRP * 8 * HD * 2, 0x00020000);
      u32 a_out = ((grp * 8 + chead) * HD + 32 * cq + 16 * lw + 2 * (lane & 7)) * 2;
      u32 oo1 = oo + 2048, oo2 = oo + 4096, oo3 = oo + 6144, a_w = (u32)(size_t)(lu32*)wbuf + (lane >> 3) * 4;
      float tv0 = bflo(tv), tv1 = bfhi(tv);
      asm volatile("" : "+v"(tv0), "+v"(tv1), "+v"(oo), "+v"(oo1), "+v"(oo2), "+v"(oo3), "+v"(a_out), "+v"(a_w));
      if (HPRIO) __builtin_amdgcn_s_setprio(0);
      __syncthreads();
      __builtin_amdgcn_s_barrier();
      __builtin_amdgcn_s_barrier();
      for (u32 it = 0; it < SPIN_CAP * 16; ++it) { if (__hip_atomic_load(gate, __ATOMIC_RELAXED, __HIP_MEMORY_SCOPE_WORKGROUP) == tag) break; }
      u32x4 og[4];
      for (u32 it = 0; it < SPIN_CAP; ++it) {
        asm volatile("" ::: "memory");
        og[0] = __builtin_amdgcn_raw_buffer_load_b128(rw, oo, 0, 16); og[1] = __builtin_amdgcn_raw_buffer_load_b128(rw, oo1, 0, 16);
        og[2] = __builtin_amdgcn_raw_buffer_load_b128(rw, oo2, 0, 16); og[3] = __builtin_amdgcn_raw_buffer_load_b128(rw, oo3, 0, 16);
        if (it == 0) { for (u32 i2 = 0; i2 < SPIN_CAP * 16; ++i2) { if (__hip_atomic_load(gate + 1, __ATOMIC_RELAXED, __HIP_MEMORY_SCOPE_WORKGROUP) == tag) break; } }
        u32 bad = 0;
#pragma unroll
        for (int i = 0; i < 4; ++i) bad |= (og[i][1] ^ tag) | (og[i][3] ^ tag);
        if (__builtin_expect(__all(bad == 0), 1)) break;
        __builtin_amdgcn_s_sleep(POLL_SLEEP);
      }
      const __attribute__((address_space(3))) float* wp = (const __attribute__((address_space(3))) float*)a_w;
      const float w0 = wp[0], w1 = wp[8], w2 = wp[16], w3 = wp[24], wt = wbuf[32];
      float a0 = fmaf(w0, asf(og[0][0]), w1 * asf(og[1][0])) + fmaf(w2, asf(og[2][0]), w3 * asf(og[3][0]));
      float a1 = fmaf(w0, asf(og[0][2]), w1 * asf(og[1][2])) + fmaf(w2, asf(og[2][2]), w3 * asf(og[3][2]));
      a0 += dpp<ROR8>(a0); a1 += dpp<ROR8>(a1);
      a0 = fmaf(wt, tv0, x32_sum(x16_sum(a0))); a1 = fmaf(wt, tv1, x32_sum(x16_sum(a1)));
      if (lane < 8) __builtin_amdgcn_raw_buffer_store_b32(cvtpk(a0, a1), ro, a_out, 0, OUT_AUX);
      if (split == 0 && lw == 0 && lane == 0) __builtin_amdgcn_raw_buffer_store_b64(u32x2{tag, (u32)(epoch >> 32)}, rw, WS_EPOCH + grp * 256, 0, 16);
    }
    return;
  }
  {
    const size_t key0 = (size_t)grp * CTX + split * KEYS_WG + wave * (ROT == 4 ? 32 : KEYS_W);
    constexpr u32 BSTR = ROT == 4 ? 32768 : 8192, WBYTES = ROT == 4 ? 3 * 32768 + 8192 : KEYS_W * HD * 2;
    const auto rk = __builtin_amdgcn_make_buffer_rsrc((void*)(k + key0 * HD), 0, WBYTES, 0x00020000);
    const u32 rot = ROT == 1 ? (split + wave) & 3 : ROT == 2 ? (split >> 3) & 3 : ROT == 3 ? wave : 0;
    const u32 sb0 = (rot & 3) * BSTR, sb1 = ((rot + 1) & 3) * BSTR, sb2 = ((rot + 2) & 3) * BSTR, sb3 = ((rot + 3) & 3) * BSTR;
#define SBLK(kb) ((kb) == 0 ? sb0 : (kb) == 1 ? sb1 : (kb) == 2 ? sb2 : sb3)
    u32x4 kf[16][2], qf[2];
    const u32 koff = (16 * ((c >> 1) & 1) + (c >> 2)) * 256 + g * 32 + (c & 1) * 16;
#define LOADK(t) do { kf[t][0] = __builtin_amdgcn_raw_buffer_load_b128(rk, koff + ((t) & 3) * 1024, SBLK((t) >> 2), KV_AUX); \
                      kf[t][1] = __builtin_amdgcn_raw_buffer_load_b128(rk, koff + ((t) & 3) * 1024 + 128, SBLK((t) >> 2), KV_AUX); } while (0)
    LOADK(0);
    SB();
    const auto rq = __builtin_amdgcn_make_buffer_rsrc((void*)(q + grp * 8 * HD), 0, 8 * HD * 2, 0x00020000);
    const auto rv = __builtin_amdgcn_make_buffer_rsrc((void*)(v + key0 * HD), 0, WBYTES, 0x00020000);
    u32x4 vf[4][8];
    const u32 voff = lane * 16;
#define LOADV(kb) _Pragma("unroll") for (int j = 0; j < 8; ++j) vf[kb][j] = __builtin_amdgcn_raw_buffer_load_b128(rv, voff + (j & 3) * 1024 + (j >> 2) * 4096 * 0, SBLK(kb) + (j >> 2) * 4096, KV_AUX)
    qf[0] = __builtin_amdgcn_raw_buffer_load_b128(rq, (c >> 1) * 256 + g * 32 + (c & 1) * 16, 0, 0);
    qf[1] = __builtin_amdgcn_raw_buffer_load_b128(rq, (c >> 1) * 256 + g * 32 + (c & 1) * 16 + 128, 0, 0);
    const u32 E = __builtin_amdgcn_raw_buffer_load_b32(rw, WS_EPOCH + grp * 256, 0, 16);
#pragma unroll
    for (int t = 1; t < 16; ++t) LOADK(t);
    LOADV(0);
    SB();
    const bool par = (c & 1) != 0;
    float s[16];
    float mx = -INFINITY;
#pragma unroll
    for (int t = 0; t < 16; ++t) {
      f32x4 d = mfma(kf[t][0], qf[0], f32x4{0.f, 0.f, 0.f, 0.f});
      d = mfma(kf[t][1], qf[1], d);
      const float ev = d[0] + dpp<XOR1>(d[1]), od = d[3] + dpp<XOR1>(d[2]);
      s[t] = par ? od : ev;
      mx = fmaxf(mx, s[t]);
    }
    mx = fmaxf(mx, dpp<XOR1>(mx)); mx = x16_max(mx); mx = x32_max(mx);
    if (lane < 16 && !par) smax[(lane >> 1) * 4 + wave] = mx;
    STAMP(8);
    SB(); LOADV(1); SB();
    __syncthreads();
    float M;
    { const f32x4 a = *(const f32x4*)(smax + (c >> 1) * 4); M = fmaxf(fmaxf(a[0], a[1]), fmaxf(a[2], a[3])); }
    const float mc = asf(round_up24(M * C2));
    float ls = 0.f;
    u32x4 pf[4];
#pragma unroll
    for (int kb = 0; kb < 4; ++kb) {
      float p[4];
#pragma unroll
      for (int i = 0; i < 4; ++i) { p[i] = __builtin_amdgcn_exp2f(fmaf(s[4 * kb + i], C2, -mc)); ls += p[i]; }
      const u32 p01 = cvtpk(p[0], p[1]), p23 = cvtpk(p[2], p[3]);
      pf[kb][0] = __builtin_amdgcn_mov_dpp(p01, 0xA0, 0xf, 0xf, true); pf[kb][1] = __builtin_amdgcn_mov_dpp(p23, 0xA0, 0xf, 0xf, true);
      pf[kb][2] = __builtin_amdgcn_mov_dpp(p01, 0xF5, 0xf, 0xf, true); pf[kb][3] = __builtin_amdgcn_mov_dpp(p23, 0xF5, 0xf, 0xf, true);
    }
    ls += dpp<XOR1>(ls); ls = x16_sum(ls); ls = x32_sum(ls);
    if (lane < 16 && !par) lsum[(lane >> 1) * 4 + wave] = ls;
    const u32 tag = E + 1u;
    u32 a_ow = ((wave * 8 + (c >> 1)) * OSTR + 32 * g + 16 * (c & 1)) * 4;
    u32 a_or = (2 * wave * OSTR + 2 * lane) * 4;
    u32 a_pub = WS_O + ((grp * NSPLIT + 8 * wave + g) * NSPLIT + split) * 256 + c * 16;
    u32 a_or1 = a_or + 8 * OSTR * 4, a_or2 = a_or + 16 * OSTR * 4, a_or3 = a_or + 24 * OSTR * 4, a_pub1 = a_pub + 4 * NSPLIT * 256;
    asm volatile("" : "+v"(a_ow), "+v"(a_or), "+v"(a_or1), "+v"(a_or2), "+v"(a_or3), "+v"(a_pub), "+v"(a_pub1));
    STAMP(1);
    SB(); LOADV(2); LOADV(3); SB();
    f32x4 acc[8];
#pragma unroll
    for (int e = 0; e < 8; ++e) acc[e] = f32x4{0.f, 0.f, 0.f, 0.f};
#define PV(kb) _Pragma("unroll") for (int e = 0; e < 8; ++e) { const u32 sel = (e & 1) ? 0x07060302u : 0x05040100u; u32x4 a; \
      _Pragma("unroll") for (int jj = 0; jj < 4; ++jj) a[jj] = __builtin_amdgcn_perm(vf[kb][2 * jj + 1][e >> 1], vf[kb][2 * jj][e >> 1], sel); \
      acc[e] = mfma(a, pf[kb], acc[e]); }
    PV(0); SB(); PV(1); SB(); PV(2); SB();
    { u32x4 a[8];
#pragma unroll
      for (int jj = 0; jj < 4; ++jj) {
        if (jj == 3) { SB(); asm volatile("s_waitcnt lgkmcnt(0)" ::: "memory"); __builtin_amdgcn_s_barrier(); SB(); }
#pragma unroll
        for (int e = 0; e < 8; ++e) a[e][jj] = __builtin_amdgcn_perm(vf[3][2 * jj + 1][e >> 1], vf[3][2 * jj][e >> 1], (e & 1) ? 0x07060302u : 0x05040100u);
        SB();
      }
#pragma unroll
      for (int e = 0; e < 8; ++e) acc[e] = mfma(a[e], pf[3], acc[e]);
    }
    STAMP(2);
#pragma unroll
    for (int rr = 0; rr < 2; ++rr)
#pragma unroll
      for (int eh = 0; eh < 2; ++eh) { f32x4 t;
#pragma unroll
        for (int e = 0; e < 4; ++e) { float lo = acc[eh * 4 + e][rr], hi = acc[eh * 4 + e][2 + rr]; asm("" : "+v"(lo)); t[e] = par ? hi : lo; }
        *(f32x4*)((char*)lds + a_ow + rr * 32 + eh * 16) = t; }
    __syncthreads();
    STAMP(9);
#pragma unroll
    for (int hh = 0; hh < 2; ++hh) {
      const f32x2 o0 = *(const f32x2*)((char*)lds + a_or + hh * OSTR * 4), o1 = *(const f32x2*)((char*)lds + a_or1 + hh * OSTR * 4),
                  o2 = *(const f32x2*)((char*)lds + a_or2 + hh * OSTR * 4), o3 = *(const f32x2*)((char*)lds + a_or3 + hh * OSTR * 4);
      float x = (o0[0] + o1[0]) + (o2[0] + o3[0]), y = (o0[1] + o1[1]) + (o2[1] + o3[1]);
      __builtin_amdgcn_raw_buffer_store_b128(u32x4{asu(x), tag, asu(y), tag}, rw, hh ? a_pub1 : a_pub, 0, 16);
    }
    STAMP(3);
  }
}
}

extern "C" void kernel_launch(void* const* d_in, const int* in_sizes, int n_in, void* d_out, int out_size, void* d_ws, size_t ws_size, hipStream_t stream) {
  (void)in_sizes; (void)n_in; (void)out_size; (void)ws_size;
  tree_attn_decode<<<dim3(NGRP * NSPLIT), dim3(512), 0, stream>>>((const uint16_t*)d_in[0], (const uint16_t*)d_in[1], (const uint16_t*)d_in[2], (const uint16_t*)d_in[3],
                                                                  (const uint16_t*)d_in[4], (const uint16_t*)d_in[5], (uint16_t*)d_out, (unsigned char*)d_ws);
}
```

```cpp
#include <hip/hip_runtime.h>
#include <stdint.h>

#define KV_AUX 2
#define POLL_SLEEP 1
#define HINT_ATOMIC 1
#define HINT_DELAY 0
#define HINT_POS 1
#define ROT 0
#define CODE_PF 6784
#define HPRIO 3
#define SPRIO 0
#define POLL_GAP 6
#define OUT_AUX 0
#define PRETOUCH 0
#define WS_BASE 0
#define STAMP(i) do {} while (0)
#define STAMP_OUT(a, b) do {} while (0)

namespace {
typedef unsigned int u32;
typedef u32 u32x4 __attribute__((ext_vector_type(4)));
typedef float f32x4 __attribute__((ext_vector_type(4)));
typedef float f32x2 __attribute__((ext_vector_type(2)));
typedef unsigned int u32x2 __attribute__((ext_vector_type(2)));
typedef __bf16 bf16x8 __attribute__((ext_vector_type(8)));
typedef unsigned long long u64;
typedef __attribute__((address_space(1))) u64 gu64;
typedef __attribute__((address_space(3))) u32 lu32;

constexpr int CTX = 16384, HD = 128, NGRP = 8, NSPLIT = 32, NWAVE = 4;
constexpr int KEYS_WG = CTX / NSPLIT, KEYS_W = KEYS_WG / NWAVE;
constexpr float QK_SCALE = 0.08838834764831845f;
constexpr float LOG2E = 1.4426950408889634f;
constexpr float C2 = QK_SCALE * LOG2E;
constexpr u32 WS_EPOCH = 0, WS_O = 4096, O_BYTES = NGRP * NSPLIT * NSPLIT * 256;
constexpr u32 WS_HINT = WS_O + O_BYTES, HINT_BYTES = NGRP * NSPLIT * 64 * 8;
constexpr u32 WS_BYTES = WS_HINT + HINT_BYTES;
constexpr int OSTR = 132;
constexpr u32 SPIN_CAP = 1u << 20;
constexpr int LDS_FLOATS = NWAVE * 8 * OSTR + 64 + 4 + 36;

__device__ __forceinline__ float asf(u32 x) { return __builtin_bit_cast(float, x); }
__device__ __forceinline__ u32 asu(float x) { return __builtin_bit_cast(u32, x); }
__device__ __forceinline__ float x16_max(float x) { auto s = __builtin_amdgcn_permlane16_swap(asu(x), asu(x), false, false); return fmaxf(asf(s[0]), asf(s[1])); }
__device__ __forceinline__ float x32_max(float x) { auto s = __builtin_amdgcn_permlane32_swap(asu(x), asu(x), false, false); return fmaxf(asf(s[0]), asf(s[1])); }
__device__ __forceinline__ float x16_sum(float x) { auto s = __builtin_amdgcn_permlane16_swap(asu(x), asu(x), false, false); return asf(s[0]) + asf(s[1]); }
__device__ __forceinline__ float x32_sum(float x) { auto s = __builtin_amdgcn_permlane32_swap(asu(x), asu(x), false, false); return asf(s[0]) + asf(s[1]); }
template <int CTRL> __device__ __forceinline__ float dpp(float x) { return asf(__builtin_amdgcn_mov_dpp((int)asu(x), CTRL, 0xf, 0xf, true)); }
constexpr int XOR1 = 0xB1, XOR2 = 0x4E, HMIR = 0x141, ROR8 = 0x128, ROR4 = 0x124;
__device__ __forceinline__ float row_sum(float x) { x += dpp<XOR1>(x); x += dpp<XOR2>(x); x += dpp<HMIR>(x); x += dpp<ROR8>(x); return x; }
__device__ __forceinline__ float hi4_sum(float x) { x += dpp<ROR8>(x); x += dpp<ROR4>(x); return x32_sum(x16_sum(x)); }
__device__ __forceinline__ float hi4_max(float x) { x = fmaxf(x, dpp<ROR8>(x)); x = fmaxf(x, dpp<ROR4>(x)); return x32_max(x16_max(x)); }
__device__ __forceinline__ u32 cvtpk(float lo, float hi) { u32 r; asm("v_cvt_pk_bf16_f32 %0, %1, %2" : "=v"(r) : "v"(lo), "v"(hi)); return r; }
__device__ __forceinline__ float bflo(u32 x) { return asf(x << 16); }
__device__ __forceinline__ float bfhi(u32 x) { return asf(x & 0xffff0000u); }
__device__ __forceinline__ f32x4 mfma(u32x4 a, u32x4 b, f32x4 c) {
  return __builtin_amdgcn_mfma_f32_16x16x32_bf16(__builtin_bit_cast(bf16x8, a), __builtin_bit_cast(bf16x8, b), c, 0, 0, 0); }
__device__ __forceinline__ u32 round_up24(float x) { const u32 u = asu(x); return (x >= 0.f ? u + 0xFFu : u) & ~0xFFu; }
#define SB() __builtin_amdgcn_sched_barrier(0)

__global__ __launch_bounds__(512, 2) void tree_attn_decode(const uint16_t* __restrict__ q, const uint16_t* __restrict__ k, const uint16_t* __restrict__ v,
                                                           const uint16_t* __restrict__ kt, const uint16_t* __restrict__ vt, const uint16_t* __restrict__ mask,
                                                           uint16_t* __restrict__ out, unsigned char* __restrict__ ws0) {
  __shared__ __attribute__((aligned(16))) float lds[LDS_FLOATS];
  float* smax = lds + NWAVE * 8 * OSTR;
  float* lsum = smax + 32;
  const int tid = threadIdx.x, lane = tid & 63, wave = __builtin_amdgcn_readfirstlane(tid >> 6);
  if (HPRIO && wave >= NWAVE) __builtin_amdgcn_s_setprio(HPRIO);
  if (CODE_PF && wave == 7) {
    u64 pc; asm volatile("s_getpc_b64 %0" : "=s"(pc));
    if (lane * 128 < CODE_PF) { const u32 x = *(const __attribute__((address_space(1))) u32*)((pc & ~255ull) + lane * 128); asm volatile("" :: "v"(x)); }
  }
  asm volatile("" :: "s"(q), "s"(k), "s"(v), "s"(kt), "s"(vt), "s"(mask), "s"(out), "s"(ws0));
  unsigned char* ws = ws0 + WS_BASE;
  const int grp = blockIdx.x & 7, split = blockIdx.x >> 3;
  const int g = lane >> 4, c = lane & 15;
  const auto rw = __builtin_amdgcn_make_buffer_rsrc((void*)ws, 0, WS_BYTES, 0x00020000);

  if (wave >= NWAVE) {
    const u32x2 E2 = __builtin_amdgcn_raw_buffer_load_b64(rw, WS_EPOCH + grp * 256, 0, 16);
    const u64 epoch = (((u64)E2[1] << 32) | E2[0]) + 1;
    const u32 tag = (u32)epoch;
    lu32* gate = (lu32*)(lds + NWAVE * 8 * OSTR + 64);
    float* wbuf = lds + NWAVE * 8 * OSTR + 64 + 4;
    const int chead = split >> 2, cq = split & 3;
    if (wave < 6) {
      u64 a_h = (u64)(ws + WS_HINT + (wave == 4 ? (grp * NSPLIT + split) * 64 + lane : (grp * NSPLIT + (lane & 31)) * 64 + (lane & 32) + split) * 8);
      float st_ = 0.f;
      if (wave == 4) {
        const u32 tq = *(const u32*)(q + (grp * 8 + chead) * HD + 2 * lane);
        const u32 tk = *(const u32*)(kt + grp * HD + 2 * lane);
        const u32 tm = mask[grp >> 2];
        float d = bflo(tq) * bflo(tk) + bfhi(tq) * bfhi(tk);
        d = row_sum(d); d = x16_sum(d); d = x32_sum(d);
        st_ = (d * QK_SCALE + (1.0f - bflo(tm)) * -1.0e9f) * LOG2E;
        if (lane < 2) __hip_atomic_store(gate + lane, ~tag, __ATOMIC_RELAXED, __HIP_MEMORY_SCOPE_WORKGROUP);
      }
      u32 a_s = ((lane & 31) >> 2) * 16;
      asm volatile("" : "+v"(a_h), "+v"(st_), "+v"(a_s));
      if (HPRIO) __builtin_amdgcn_s_setprio(0);
      __syncthreads();
      __builtin_amdgcn_s_barrier();
      u64 word = 0;
      if (wave == 5) {
        const f32x4 a = *(const f32x4*)((const char*)smax + a_s), b = *(const f32x4*)((const char*)lsum + a_s);
        const u32 mb = round_up24(fmaxf(fmaxf(a[0], a[1]), fmaxf(a[2], a[3])) * C2), lb = asu((b[0] + b[1]) + (b[2] + b[3])) + 0x80u;
        word = (epoch << 24) | ((lane < 32 ? mb : lb) >> 8);
        asm volatile("" : "+v"(word));
        if (HINT_POS == 0) {
          if (HINT_DELAY) __builtin_amdgcn_s_sleep(HINT_DELAY);
          (void)__hip_atomic_exchange((gu64*)a_h, word, __ATOMIC_RELAXED, __HIP_MEMORY_SCOPE_AGENT);
          return;
        }
      }
      __builtin_amdgcn_s_barrier();
      if (wave == 5) {
        if (HINT_DELAY) __builtin_amdgcn_s_sleep(HINT_DELAY);
        (void)__hip_atomic_exchange((gu64*)a_h, word, __ATOMIC_RELAXED, __HIP_MEMORY_SCOPE_AGENT);
      } else {
        STAMP(5);
        const u32x4 cd = {~tag, ~tag, ~tag, ~tag};
        const u64 want = epoch & 0xFFFFFFFFFFull;
        u64 r0, r1, r2, win, tmp; u32 cnt;
        const u32 a_gate = (u32)(size_t)gate;
#define STR2(x) #x
#define STR(x) STR2(x)
#define POLL_STEP(r) \
            "s_waitcnt vmcnt(2)\n\t" \
            "v_lshrrev_b64 %[tmp], 24, %[" r "]\n\t" \
            "v_mov_b64 %[win], %[" r "]\n\t" \
            "v_cmp_eq_u64 vcc, %[tmp], %[want]\n\t" \
            "s_cmp_eq_u64 vcc, exec\n\t" \
            "s_cbranch_scc1 2f\n\t" \
            "global_atomic_cmpswap_x2 %[" r "], %[a], %[cd], off sc0\n\t"
        asm volatile(
            "s_mov_b32 %[cnt], 0x100000\n\t"
            "global_atomic_cmpswap_x2 %[r0], %[a], %[cd], off sc0\n\t"
            "s_sleep " STR(POLL_GAP) "\n\t"
            "global_atomic_cmpswap_x2 %[r1], %[a], %[cd], off sc0\n\t"
            "s_sleep " STR(POLL_GAP) "\n\t"
            "global_atomic_cmpswap_x2 %[r2], %[a], %[cd], off sc0\n"
            "1:\n\t"
            POLL_STEP("r0") POLL_STEP("r1") POLL_STEP("r2")
            "s_sub_u32 %[cnt], %[cnt], 1\n\t"
            "s_cmp_lg_u32 %[cnt], 0\n\t"
            "s_cbranch_scc1 1b\n"
            "2:\n\t"
            "ds_write_b32 %[ga], %[tag]"
            : [r0] "=&v"(r0), [r1] "=&v"(r1), [r2] "=&v"(r2), [win] "=&v"(win), [tmp] "=&v"(tmp), [cnt] "=&s"(cnt)
            : [a] "v"(a_h), [cd] "v"(cd), [want] "v"(want), [ga] "v"(a_gate), [tag] "v"(tag)
            : "vcc", "scc", "memory");
        STAMP(6);
        const float x = asf((u32)win << 8);
        float Mx = lane < 32 ? x : st_;
        Mx = fmaxf(Mx, dpp<XOR1>(Mx)); Mx = fmaxf(Mx, dpp<XOR2>(Mx)); Mx = fmaxf(Mx, dpp<HMIR>(Mx)); Mx = fmaxf(Mx, dpp<ROR8>(Mx)); Mx = x32_max(x16_max(Mx));
        const auto sw = __builtin_amdgcn_permlane32_swap(asu(x), asu(x), false, false);
        const float w = __builtin_amdgcn_exp2f((lane < 32 ? x : st_) - Mx);
        float L = lane < 32 ? w * asf(sw[1]) : 0.f;
        L = row_sum(L); L = x32_sum(x16_sum(L));
        const auto wsw = __builtin_amdgcn_permlane32_swap(asu(w), asu(w), false, false);
        L += asf(wsw[1]);
        const float wn = w * __builtin_amdgcn_rcpf(L);
        if (lane < 33) wbuf[lane] = wn;
        asm volatile("s_waitcnt lgkmcnt(0)" ::: "memory");
        if (lane == 0) __hip_atomic_store(gate + 1, tag, __ATOMIC_RELAXED, __HIP_MEMORY_SCOPE_WORKGROUP);
        asm volatile("s_waitcnt vmcnt(0)" : "+v"(r0), "+v"(r1), "+v"(r2) :: "memory");
        STAMP_OUT(5, 7);
      }
    } else {
      const int lw = wave - 6;
      const u32 tv = *(const u32*)(vt + grp * HD + 32 * cq + 16 * lw + 2 * (lane & 7));
      u32 oo = WS_O + ((grp * NSPLIT + split) * NSPLIT + (lane >> 3)) * 256 + lw * 128 + (lane & 7) * 16;
      const auto ro = __builtin_amdgcn_make_buffer_rsrc((void*)out, 0, NGRP * 8 * HD * 2, 0x00020000);
      u32 a_out = ((grp * 8 + chead) * HD + 32 * cq + 16 * lw + 2 * (lane & 7)) * 2;
      u32 oo1 = oo + 2048, oo2 = oo + 4096, oo3 = oo + 6144, a_w = (u32)(size_t)(lu32*)wbuf + (lane >> 3) * 4;
      float tv0 = bflo(tv), tv1 = bfhi(tv);
      asm volatile("" : "+v"(tv0), "+v"(tv1), "+v"(oo), "+v"(oo1), "+v"(oo2), "+v"(oo3), "+v"(a_out), "+v"(a_w));
      if (HPRIO) __builtin_amdgcn_s_setprio(0);
      __syncthreads();
      __builtin_amdgcn_s_barrier();
      __builtin_amdgcn_s_barrier();
      for (u32 it = 0; it < SPIN_CAP * 16; ++it) { if (__hip_atomic_load(gate, __ATOMIC_RELAXED, __HIP_MEMORY_SCOPE_WORKGROUP) == tag) break; }
      u32x4 og[4];
      for (u32 it = 0; it < SPIN_CAP; ++it) {
        asm volatile("" ::: "memory");
        og[0] = __builtin_amdgcn_raw_buffer_load_b128(rw, oo, 0, 16); og[1] = __builtin_amdgcn_raw_buffer_load_b128(rw, oo1, 0, 16);
        og[2] = __builtin_amdgcn_raw_buffer_load_b128(rw, oo2, 0, 16); og[3] = __builtin_amdgcn_raw_buffer_load_b128(rw, oo3, 0, 16);
        if (it == 0) { for (u32 i2 = 0; i2 < SPIN_CAP * 16; ++i2) { if (__hip_atomic_load(gate + 1, __ATOMIC_RELAXED, __HIP_MEMORY_SCOPE_WORKGROUP) == tag) break; } }
        u32 bad = 0;
#pragma unroll
        for (int i = 0; i < 4; ++i) bad |= (og[i][1] ^ tag) | (og[i][3] ^ tag);
        if (__builtin_expect(__all(bad == 0), 1)) break;
        __builtin_amdgcn_s_sleep(POLL_SLEEP);
      }
      const __attribute__((address_space(3))) float* wp = (const __attribute__((address_space(3))) float*)a_w;
      const float w0 = wp[0], w1 = wp[8], w2 = wp[16], w3 = wp[24], wt = wbuf[32];
      float a0 = fmaf(w0, asf(og[0][0]), w1 * asf(og[1][0])) + fmaf(w2, asf(og[2][0]), w3 * asf(og[3][0]));
      float a1 = fmaf(w0, asf(og[0][2]), w1 * asf(og[1][2])) + fmaf(w2, asf(og[2][2]), w3 * asf(og[3][2]));
      a0 += dpp<ROR8>(a0); a1 += dpp<ROR8>(a1);
      a0 = fmaf(wt, tv0, x32_sum(x16_sum(a0))); a1 = fmaf(wt, tv1, x32_sum(x16_sum(a1)));
      if (lane < 8) {
        if (OUT_AUX >= 0) __builtin_amdgcn_raw_buffer_store_b32(cvtpk(a0, a1), ro, a_out, 0, OUT_AUX >= 0 ? OUT_AUX : 0);
        else (void)__hip_atomic_exchange((__attribute__((address_space(1))) u32*)((char*)out + a_out), cvtpk(a0, a1), __ATOMIC_RELAXED, __HIP_MEMORY_SCOPE_AGENT);
      }
      if (split == 0 && lw == 0 && lane == 0) __builtin_amdgcn_raw_buffer_store_b64(u32x2{tag, (u32)(epoch >> 32)}, rw, WS_EPOCH + grp * 256, 0, 16);
    }
    return;
  }
  {
    const size_t key0 = (size_t)grp * CTX + split * KEYS_WG + wave * (ROT == 4 ? 32 : KEYS_W);
    constexpr u32 BSTR = ROT == 4 ? 32768 : 8192, WBYTES = ROT == 4 ? 3 * 32768 + 8192 : KEYS_W * HD * 2;
    const auto rk = __builtin_amdgcn_make_buffer_rsrc((void*)(k + key0 * HD), 0, WBYTES, 0x00020000);
    const u32 rot = ROT == 1 ? (split + wave) & 3 : ROT == 2 ? (split >> 3) & 3 : ROT == 3 ? wave : 0;
    const u32 sb0 = (rot & 3) * BSTR, sb1 = ((rot + 1) & 3) * BSTR, sb2 = ((rot + 2) & 3) * BSTR, sb3 = ((rot + 3) & 3) * BSTR;
#define SBLK(kb) ((kb) == 0 ? sb0 : (kb) == 1 ? sb1 : (kb) == 2 ? sb2 : sb3)
    u32x4 kf[16][2], qf[2];
    const u32 koff = (16 * ((c >> 1) & 1) + (c >> 2)) * 256 + g * 32 + (c & 1) * 16;
#define LOADK(t) do { kf[t][0] = __builtin_amdgcn_raw_buffer_load_b128(rk, koff + ((t) & 3) * 1024, SBLK((t) >> 2), KV_AUX); \
                      kf[t][1] = __builtin_amdgcn_raw_buffer_load_b128(rk, koff + ((t) & 3) * 1024 + 128, SBLK((t) >> 2), KV_AUX); } while (0)
    LOADK(0);
    SB();
    const auto rq = __builtin_amdgcn_make_buffer_rsrc((void*)(q + grp * 8 * HD), 0, 8 * HD * 2, 0x00020000);
    const auto rv = __builtin_amdgcn_make_buffer_rsrc((void*)(v + key0 * HD), 0, WBYTES, 0x00020000);
    u32x4 vf[4][8];
    const u32 voff = lane * 16;
#define LOADV(kb) _Pragma("unroll") for (int j = 0; j < 8; ++j) vf[kb][j] = __builtin_amdgcn_raw_buffer_load_b128(rv, voff + (j & 3) * 1024 + (j >> 2) * 4096 * 0, SBLK(kb) + (j >> 2) * 4096, KV_AUX)
    qf[0] = __builtin_amdgcn_raw_buffer_load_b128(rq, (c >> 1) * 256 + g * 32 + (c & 1) * 16, 0, 0);
    qf[1] = __builtin_amdgcn_raw_buffer_load_b128(rq, (c >> 1) * 256 + g * 32 + (c & 1) * 16 + 128, 0, 0);
    const u32 E = __builtin_amdgcn_raw_buffer_load_b32(rw, WS_EPOCH + grp * 256, 0, 16);
#pragma unroll
    for (int t = 1; t < 16; ++t) LOADK(t);
    LOADV(0);
    SB();
    const bool par = (c & 1) != 0;
    float s[16];
    float mx = -INFINITY;
#pragma unroll
    for (int t = 0; t < 16; ++t) {
      f32x4 d = mfma(kf[t][0], qf[0], f32x4{0.f, 0.f, 0.f, 0.f});
      d = mfma(kf[t][1], qf[1], d);
      const float ev = d[0] + dpp<XOR1>(d[1]), od = d[3] + dpp<XOR1>(d[2]);
      s[t] = par ? od : ev;
      mx = fmaxf(mx, s[t]);
    }
    mx = fmaxf(mx, dpp<XOR1>(mx)); mx = x16_max(mx); mx = x32_max(mx);
    if (lane < 16 && !par) smax[(lane >> 1) * 4 + wave] = mx;
    STAMP(8);
    SB(); LOADV(1); SB();
    __syncthreads();
    float M;
    { const f32x4 a = *(const f32x4*)(smax + (c >> 1) * 4); M = fmaxf(fmaxf(a[0], a[1]), fmaxf(a[2], a[3])); }
    const float mc = asf(round_up24(M * C2));
    float ls = 0.f;
    u32x4 pf[4];
#pragma unroll
    for (int kb = 0; kb < 4; ++kb) {
      float p[4];
#pragma unroll
      for (int i = 0; i < 4; ++i) { p[i] = __builtin_amdgcn_exp2f(fmaf(s[4 * kb + i], C2, -mc)); ls += p[i]; }
      const u32 p01 = cvtpk(p[0], p[1]), p23 = cvtpk(p[2], p[3]);
      pf[kb][0] = __builtin_amdgcn_mov_dpp(p01, 0xA0, 0xf, 0xf, true); pf[kb][1] = __builtin_amdgcn_mov_dpp(p23, 0xA0, 0xf, 0xf, true);
      pf[kb][2] = __builtin_amdgcn_mov_dpp(p01, 0xF5, 0xf, 0xf, true); pf[kb][3] = __builtin_amdgcn_mov_dpp(p23, 0xF5, 0xf, 0xf, true);
    }
    ls += dpp<XOR1>(ls); ls = x16_sum(ls); ls = x32_sum(ls);
    if (lane < 16 && !par) lsum[(lane >> 1) * 4 + wave] = ls;
    const u32 tag = E + 1u;
    u32 a_ow = ((wave * 8 + (c >> 1)) * OSTR + 32 * g + 16 * (c & 1)) * 4;
    u32 a_or = (2 * wave * OSTR + 2 * lane) * 4;
    u32 a_pub = WS_O + ((grp * NSPLIT + 8 * wave + g) * NSPLIT + split) * 256 + c * 16;
    u32 a_or1 = a_or + 8 * OSTR * 4, a_or2 = a_or + 16 * OSTR * 4, a_or3 = a_or + 24 * OSTR * 4, a_pub1 = a_pub + 4 * NSPLIT * 256;
    asm volatile("" : "+v"(a_ow), "+v"(a_or), "+v"(a_or1), "+v"(a_or2), "+v"(a_or3), "+v"(a_pub), "+v"(a_pub1));
    STAMP(1);
    SB(); LOADV(2); LOADV(3); SB();
    f32x4 acc[8];
#pragma unroll
    for (int e = 0; e < 8; ++e) acc[e] = f32x4{0.f, 0.f, 0.f, 0.f};
#define PV(kb) _Pragma("unroll") for (int e = 0; e < 8; ++e) { const u32 sel = (e & 1) ? 0x07060302u : 0x05040100u; u32x4 a; \
      _Pragma("unroll") for (int jj = 0; jj < 4; ++jj) a[jj] = __builtin_amdgcn_perm(vf[kb][2 * jj + 1][e >> 1], vf[kb][2 * jj][e >> 1], sel); \
      acc[e] = mfma(a, pf[kb], acc[e]); }
    if (SPRIO) __builtin_amdgcn_s_setprio(SPRIO);
    PV(0); SB(); PV(1); SB(); PV(2); SB();
    { u32x4 a[8];
#pragma unroll
      for (int jj = 0; jj < 4; ++jj) {
        if (jj == 3) { SB(); asm volatile("s_waitcnt lgkmcnt(0)" ::: "memory"); __builtin_amdgcn_s_barrier(); SB(); }
#pragma unroll
        for (int e = 0; e < 8; ++e) a[e][jj] = __builtin_amdgcn_perm(vf[3][2 * jj + 1][e >> 1], vf[3][2 * jj][e >> 1], (e & 1) ? 0x07060302u : 0x05040100u);
        SB();
      }
#pragma unroll
      for (int e = 0; e < 8; ++e) acc[e] = mfma(a[e], pf[3], acc[e]);
    }
    STAMP(2);
#pragma unroll
    for (int rr = 0; rr < 2; ++rr)
#pragma unroll
      for (int eh = 0; eh < 2; ++eh) { f32x4 t;
#pragma unroll
        for (int e = 0; e < 4; ++e) { float lo = acc[eh * 4 + e][rr], hi = acc[eh * 4 + e][2 + rr]; asm("" : "+v"(lo)); t[e] = par ? hi : lo; }
        *(f32x4*)((char*)lds + a_ow + rr * 32 + eh * 16) = t; }
    __syncthreads();
    STAMP(9);
#pragma unroll
    for (int hh = 0; hh < 2; ++hh) {
      const f32x2 o0 = *(const f32x2*)((char*)lds + a_or + hh * OSTR * 4), o1 = *(const f32x2*)((char*)lds + a_or1 + hh * OSTR * 4),
                  o2 = *(const f32x2*)((char*)lds + a_or2 + hh * OSTR * 4), o3 = *(const f32x2*)((char*)lds + a_or3 + hh * OSTR * 4);
      float x = (o0[0] + o1[0]) + (o2[0] + o3[0]), y = (o0[1] + o1[1]) + (o2[1] + o3[1]);
      __builtin_amdgcn_raw_buffer_store_b128(u32x4{asu(x), tag, asu(y), tag}, rw, hh ? a_pub1 : a_pub, 0, 16);
    }
    STAMP(3);
  }
}
}

extern "C" void kernel_launch(void* const* d_in, const int* in_sizes, int n_in, void* d_out, int out_size, void* d_ws, size_t ws_size, hipStream_t stream) {
  (void)in_sizes; (void)n_in; (void)out_size; (void)ws_size;
  tree_attn_decode<<<dim3(NGRP * NSPLIT), dim3(512), 0, stream>>>((const uint16_t*)d_in[0], (const uint16_t*)d_in[1], (const uint16_t*)d_in[2], (const uint16_t*)d_in[3],
                                                                  (const uint16_t*)d_in[4], (const uint16_t*)d_in[5], (uint16_t*)d_out, (unsigned char*)d_ws);
}
```

```cpp
#include <hip/hip_runtime.h>
#include <stdint.h>

namespace {
typedef unsigned int u32;
typedef unsigned long long u64;
typedef u32 u32x2 __attribute__((ext_vector_type(2)));
typedef u32 u32x4 __attribute__((ext_vector_type(4)));
typedef float f32x2 __attribute__((ext_vector_type(2)));
typedef float f32x4 __attribute__((ext_vector_type(4)));
typedef __bf16 bf16x8 __attribute__((ext_vector_type(8)));
typedef __attribute__((address_space(1))) u64 gu64;
typedef __attribute__((address_space(1))) u32 gu32;
typedef __attribute__((address_space(3))) u32 lu32;
typedef __attribute__((address_space(3))) float lf32;

constexpr int CTX = 16384, HD = 128, NGRP = 8, NSPLIT = 32, NWAVE = 4;
constexpr int KEYS_WG = CTX / NSPLIT, KEYS_W = KEYS_WG / NWAVE;
constexpr float QK_SCALE = 0.08838834764831845f;
constexpr float LOG2E = 1.4426950408889634f;
constexpr float C2 = QK_SCALE * LOG2E;
constexpr int AUX_NT = 2, AUX_SC1 = 16;
constexpr u32 WS_EPOCH = 0, WS_O = 4096, O_BYTES = NGRP * NSPLIT * NSPLIT * 128;
constexpr u32 WS_STAT = WS_O + O_BYTES, STAT_BYTES = NGRP * NSPLIT * 64 * 8;
constexpr u32 WS_BYTES = WS_STAT + STAT_BYTES;
constexpr int OSTR = 132;
constexpr int L_MAX = NWAVE * 8 * OSTR, L_SUM = L_MAX + 32, L_F = L_SUM + 32, L_GATE = L_F + 32, L_W = L_GATE + 4, LDS_FLOATS = L_W + 36;
constexpr u32 SPIN_CAP = 1u << 20;
constexpr int CODE_BYTES = 7168;

__device__ __forceinline__ float asf(u32 x) { return __builtin_bit_cast(float, x); }
__device__ __forceinline__ u32 asu(float x) { return __builtin_bit_cast(u32, x); }
__device__ __forceinline__ float x16_max(float x) { auto s = __builtin_amdgcn_permlane16_swap(asu(x), asu(x), false, false); return fmaxf(asf(s[0]), asf(s[1])); }
__device__ __forceinline__ float x32_max(float x) { auto s = __builtin_amdgcn_permlane32_swap(asu(x), asu(x), false, false); return fmaxf(asf(s[0]), asf(s[1])); }
__device__ __forceinline__ float x16_sum(float x) { auto s = __builtin_amdgcn_permlane16_swap(asu(x), asu(x), false, false); return asf(s[0]) + asf(s[1]); }
__device__ __forceinline__ float x32_sum(float x) { auto s = __builtin_amdgcn_permlane32_swap(asu(x), asu(x), false, false); return asf(s[0]) + asf(s[1]); }
template <int CTRL> __device__ __forceinline__ float dpp(float x) { return asf(__builtin_amdgcn_mov_dpp((int)asu(x), CTRL, 0xf, 0xf, true)); }
constexpr int XOR1 = 0xB1, XOR2 = 0x4E, HMIR = 0x141, ROR8 = 0x128, ROR4 = 0x124;
__device__ __forceinline__ float row_sum(float x) { x += dpp<XOR1>(x); x += dpp<XOR2>(x); x += dpp<HMIR>(x); x += dpp<ROR8>(x); return x; }
__device__ __forceinline__ float row_max(float x) { x = fmaxf(x, dpp<XOR1>(x)); x = fmaxf(x, dpp<XOR2>(x)); x = fmaxf(x, dpp<HMIR>(x)); return fmaxf(x, dpp<ROR8>(x)); }
__device__ __forceinline__ u32 cvtpk(float lo, float hi) { u32 r; asm("v_cvt_pk_bf16_f32 %0, %1, %2" : "=v"(r) : "v"(lo), "v"(hi)); return r; }
__device__ __forceinline__ float bflo(u32 x) { return asf(x << 16); }
__device__ __forceinline__ float bfhi(u32 x) { return asf(x & 0xffff0000u); }
__device__ __forceinline__ f32x4 mfma(u32x4 a, u32x4 b, f32x4 c) {
  return __builtin_amdgcn_mfma_f32_16x16x32_bf16(__builtin_bit_cast(bf16x8, a), __builtin_bit_cast(bf16x8, b), c, 0, 0, 0); }
__device__ __forceinline__ u32 round_up24(float x) { const u32 u = asu(x); return (x >= 0.f ? u + 0xFFu : u) & ~0xFFu; }
__device__ __forceinline__ void pack_granule(float v0, float v1, u32 tag20, u32& lo, u32& hi) {
  const u32 r0 = asu(v0) + 0x200u, r1 = asu(v1) + 0x200u;
  hi = (r0 & 0xFFFFFC00u) | (r1 >> 22);
  lo = ((r1 << 10) & 0xFFF00000u) | tag20;
}
__device__ __forceinline__ float granule_v0(u32 lo, u32 hi) { (void)lo; return asf(hi & 0xFFFFFC00u); }
__device__ __forceinline__ float granule_v1(u32 lo, u32 hi) { return asf(__builtin_amdgcn_alignbit(hi, lo, 10) & 0xFFFFFC00u); }
__device__ __forceinline__ float mul1(float a, float b) { float r; asm("v_mul_f32 %0, %1, %2" : "=v"(r) : "v"(a), "v"(b)); return r; }
__device__ __forceinline__ float fma1(float a, float b, float c) { float r; asm("v_fma_f32 %0, %1, %2, %3" : "=v"(r) : "v"(a), "v"(b), "v"(c)); return r; }
#define SB() __builtin_amdgcn_sched_barrier(0)
#define LDS_WAIT_EQ(p, val) do { for (u32 it_ = 0; it_ < SPIN_CAP * 16; ++it_) if (__hip_atomic_load((p), __ATOMIC_RELAXED, __HIP_MEMORY_SCOPE_WORKGROUP) == (val)) break; } while (0)

__global__ __launch_bounds__(512, 2) void tree_attn_decode(const uint16_t* __restrict__ q, const uint16_t* __restrict__ k, const uint16_t* __restrict__ v,
                                                           const uint16_t* __restrict__ kt, const uint16_t* __restrict__ vt, const uint16_t* __restrict__ mask,
                                                           uint16_t* __restrict__ out, unsigned char* __restrict__ ws) {
  __shared__ __attribute__((aligned(16))) float lds[LDS_FLOATS];
  float* smax = lds + L_MAX;
  float* lsum = lds + L_SUM;
  const int tid = threadIdx.x, lane = tid & 63, wave = __builtin_amdgcn_readfirstlane(tid >> 6);
  u32 code_word = 0;
  if (wave >= NWAVE) {
    __builtin_amdgcn_s_setprio(3);
    u64 pc; asm volatile("s_getpc_b64 %0" : "=s"(pc));
    if (wave == 7 && lane * 128 < CODE_BYTES) code_word = *(const gu32*)((pc & ~255ull) + lane * 128);
  }
  asm volatile("" :: "s"(q), "s"(k), "s"(v), "s"(kt), "s"(vt), "s"(mask), "s"(out), "s"(ws));
  const int grp = blockIdx.x & 7, split = blockIdx.x >> 3;
  const int g = lane >> 4, c = lane & 15;
  const auto rw = __builtin_amdgcn_make_buffer_rsrc((void*)ws, 0, WS_BYTES, 0x00020000);

  if (wave >= NWAVE) {
    const u32x2 e2 = __builtin_amdgcn_raw_buffer_load_b64(rw, WS_EPOCH + grp * 256, 0, AUX_SC1);
#define EPOCH() const u64 epoch = (((u64)e2[1] << 32) | e2[0]) + 1; const u32 tag = (u32)epoch
    lu32* gate = (lu32*)(lds + L_GATE);
    float* wbuf = lds + L_W;
    const int chead = split >> 2, cq = split & 3;
    if (wave < 6) {
      u64 a_h = (u64)(ws + WS_STAT + (wave == 4 ? (grp * NSPLIT + split) * 64 + lane : (grp * NSPLIT + (lane >> 1)) * 64 + 2 * split + (lane & 1)) * 8);
      const u32 tq = *(const u32*)(q + (grp * 8 + chead) * HD + 2 * lane);
      const u32 tk = *(const u32*)(kt + grp * HD + 2 * lane);
      const u32 tm = mask[grp >> 2];
      SB();
      EPOCH();
      float st = 0.f;
      if (wave == 4) {
        float d = bflo(tq) * bflo(tk) + bfhi(tq) * bfhi(tk);
        d = x32_sum(x16_sum(row_sum(d)));
        st = (d * QK_SCALE + (1.0f - bflo(tm)) * -1.0e9f) * LOG2E;
        if (lane < 2) __hip_atomic_store(gate + lane, ~tag, __ATOMIC_RELAXED, __HIP_MEMORY_SCOPE_WORKGROUP);
      }
      u32 a_s = (lane >> 3) * 16;
      asm volatile("" : "+v"(a_h), "+v"(st), "+v"(a_s));
      __builtin_amdgcn_s_setprio(0);
      __syncthreads();
      u64 word = 0;
      if (wave == 5) {
        const f32x4 a = *(const f32x4*)((const char*)smax + a_s), b = *(const f32x4*)((const char*)lsum + a_s);
        const u32 mb = round_up24(fmaxf(fmaxf(a[0], a[1]), fmaxf(a[2], a[3])));
        const float m = asf(mb);
        const f32x4 f = {__builtin_amdgcn_exp2f(a[0] - m), __builtin_amdgcn_exp2f(a[1] - m), __builtin_amdgcn_exp2f(a[2] - m), __builtin_amdgcn_exp2f(a[3] - m)};
        if ((lane & 7) == 0) *(f32x4*)((char*)(lds + L_F) + a_s) = f;
        const u32 lb = asu(fmaf(f[0], b[0], f[1] * b[1]) + fmaf(f[2], b[2], f[3] * b[3])) + 0x80u;
        word = (epoch << 24) | (((lane & 1) ? lb : mb) >> 8);
        asm volatile("s_waitcnt lgkmcnt(0)" : "+v"(word) :: "memory");
      }
      __builtin_amdgcn_s_barrier();
      if (wave == 5) (void)__hip_atomic_exchange((gu64*)a_h, word, __ATOMIC_RELAXED, __HIP_MEMORY_SCOPE_AGENT);
      if (wave == 4) {
        const u32x4 cd = {~tag, ~tag, ~tag, ~tag};
        const u64 want = epoch & 0xFFFFFFFFFFull;
        u64 r0, r1, r2, win, tmp, sv; u32 cnt;
        const u32 a_gate = (u32)(size_t)gate;
#define POLL_STEP(r) \
            "s_waitcnt vmcnt(2)\n\t" \
            "v_lshrrev_b64 %[tmp], 24, %[" r "]\n\t" \
            "v_cmp_eq_u64 vcc, %[tmp], %[want]\n\t" \
            "s_mov_b64 %[sv], exec\n\t" \
            "s_mov_b64 exec, vcc\n\t" \
            "v_mov_b64 %[win], %[" r "]\n\t" \
            "s_andn2_b64 exec, %[sv], vcc\n\t" \
            "s_cbranch_scc0 2f\n\t" \
            "global_atomic_cmpswap_x2 %[" r "], %[a], %[cd], off sc0\n\t"
        asm volatile(
            "s_mov_b32 %[cnt], 0x100000\n\t"
            "global_atomic_cmpswap_x2 %[r0], %[a], %[cd], off sc0\n\t"
            "s_sleep 6\n\t"
            "global_atomic_cmpswap_x2 %[r1], %[a], %[cd], off sc0\n\t"
            "s_sleep 6\n\t"
            "global_atomic_cmpswap_x2 %[r2], %[a], %[cd], off sc0\n"
            "1:\n\t"
            POLL_STEP("r0") POLL_STEP("r1") POLL_STEP("r2")
            "s_sub_u32 %[cnt], %[cnt], 1\n\t"
            "s_cmp_lg_u32 %[cnt], 0\n\t"
            "s_cbranch_scc1 1b\n"
            "2:\n\t"
            "s_mov_b64 exec, -1\n\t"
            "ds_write_b32 %[ga], %[tag]"
            : [r0] "=&v"(r0), [r1] "=&v"(r1), [r2] "=&v"(r2), [win] "=&v"(win), [tmp] "=&v"(tmp), [cnt] "=&s"(cnt), [sv] "=&s"(sv)
            : [a] "v"(a_h), [cd] "v"(cd), [want] "v"(want), [ga] "v"(a_gate), [tag] "v"(tag)
            : "vcc", "scc", "memory");
        const bool isl = (lane & 1) != 0;
        const float x = asf((u32)win << 8), xo = dpp<XOR1>(x);
        const float mx = x32_max(x16_max(row_max(isl ? st : x)));
        const float w = __builtin_amdgcn_exp2f(x - mx), wt = __builtin_amdgcn_exp2f(st - mx);
        const float wl = isl ? 0.f : w * xo;
        const float L = x32_sum(x16_sum(row_sum(wl))) + wt;
        const float inv = __builtin_amdgcn_rcpf(L);
        if (!isl) wbuf[lane >> 1] = w * inv;
        if (lane == 1) wbuf[32] = wt * inv;
        asm volatile("s_waitcnt lgkmcnt(0)" ::: "memory");
        if (lane == 0) __hip_atomic_store(gate + 1, tag, __ATOMIC_RELAXED, __HIP_MEMORY_SCOPE_WORKGROUP);
        if (split == 0 && lane == 0) __builtin_amdgcn_raw_buffer_store_b64(u32x2{tag, (u32)(epoch >> 32)}, rw, WS_EPOCH + grp * 256, 0, AUX_SC1);
        asm volatile("s_waitcnt vmcnt(0)" : "+v"(r0), "+v"(r1), "+v"(r2) :: "memory");
      }
    } else {
      const int lw = wave - 6, row = lane >> 4, dsel = (row & 1) * 2 + (row >> 1);
      const u32 tv = vt[grp * HD + 32 * cq + 16 * lw + 4 * (lane & 3) + dsel];
      SB();
      EPOCH();
      const auto ro = __builtin_amdgcn_make_buffer_rsrc((void*)out, 0, NGRP * 8 * HD * 2, 0x00020000);
      u32 oo = WS_O + ((grp * NSPLIT + split) * NSPLIT + (lane >> 2)) * 128 + lw * 64 + (lane & 3) * 16;
      u32 oo1 = oo + 16 * 128, a_w = (u32)(size_t)(lu32*)wbuf + (lane >> 2) * 4;
      u32 a_out = ((grp * 8 + chead) * HD + 32 * cq + 16 * lw + 4 * (lane & 3) + dsel) * 2;
      float tvf = bflo(tv);
      u32 tag20 = tag & 0xFFFFFu;
      asm volatile("" : "+v"(tvf), "+v"(oo), "+v"(oo1), "+v"(a_out), "+v"(a_w), "+v"(tag20) : "v"(code_word));
      __builtin_amdgcn_s_setprio(0);
      __syncthreads();
      __builtin_amdgcn_s_barrier();
      LDS_WAIT_EQ(gate, tag);
      u32x4 o0, o1;
#define LOAD_O() do { o0 = __builtin_amdgcn_raw_buffer_load_b128(rw, oo, 0, AUX_SC1); o1 = __builtin_amdgcn_raw_buffer_load_b128(rw, oo1, 0, AUX_SC1); } while (0)
#define O_STALE() ((((o0[0] ^ tag20) | (o0[2] ^ tag20) | (o1[0] ^ tag20) | (o1[2] ^ tag20)) & 0xFFFFFu) != 0)
      LOAD_O();
      SB();
      LDS_WAIT_EQ(gate + 1, tag);
      const lf32* wp = (const lf32*)(size_t)a_w;
      float w0 = wp[0], w1 = wp[16], wt = wbuf[32];
      asm volatile("" : "+v"(w0), "+v"(w1), "+v"(wt));
      SB();
      if (__builtin_expect(__any(O_STALE()), 0)) {
        for (u32 it = 0; it < SPIN_CAP; ++it) {
          if (O_STALE()) {
            u64 x0 = ~0ull, x1 = ~0ull, x2 = ~0ull, x3 = ~0ull;
            __hip_atomic_compare_exchange_strong((gu64*)(ws + oo), &x0, x0, __ATOMIC_RELAXED, __ATOMIC_RELAXED, __HIP_MEMORY_SCOPE_AGENT);
            __hip_atomic_compare_exchange_strong((gu64*)(ws + oo + 8), &x1, x1, __ATOMIC_RELAXED, __ATOMIC_RELAXED, __HIP_MEMORY_SCOPE_AGENT);
            __hip_atomic_compare_exchange_strong((gu64*)(ws + oo1), &x2, x2, __ATOMIC_RELAXED, __ATOMIC_RELAXED, __HIP_MEMORY_SCOPE_AGENT);
            __hip_atomic_compare_exchange_strong((gu64*)(ws + oo1 + 8), &x3, x3, __ATOMIC_RELAXED, __ATOMIC_RELAXED, __HIP_MEMORY_SCOPE_AGENT);
            o0 = u32x4{(u32)x0, (u32)(x0 >> 32), (u32)x1, (u32)(x1 >> 32)};
            o1 = u32x4{(u32)x2, (u32)(x2 >> 32), (u32)x3, (u32)(x3 >> 32)};
          }
          if (!__any(O_STALE())) break;
        }
      }
      float a0 = fma1(w1, granule_v0(o1[0], o1[1]), mul1(w0, granule_v0(o0[0], o0[1]))), a1 = fma1(w1, granule_v1(o1[0], o1[1]), mul1(w0, granule_v1(o0[0], o0[1])));
      float a2 = fma1(w1, granule_v0(o1[2], o1[3]), mul1(w0, granule_v0(o0[2], o0[3]))), a3 = fma1(w1, granule_v1(o1[2], o1[3]), mul1(w0, granule_v1(o0[2], o0[3])));
      asm volatile("s_nop 1" : "+v"(a0), "+v"(a1), "+v"(a2), "+v"(a3));
      const auto s01 = __builtin_amdgcn_permlane32_swap(asu(a0), asu(a1), false, false), s23 = __builtin_amdgcn_permlane32_swap(asu(a2), asu(a3), false, false);
      const float b01 = asf(s01[0]) + asf(s01[1]), b23 = asf(s23[0]) + asf(s23[1]);
      const auto sr = __builtin_amdgcn_permlane16_swap(asu(b01), asu(b23), false, false);
      float r = asf(sr[0]) + asf(sr[1]);
      r += dpp<ROR8>(r); r += dpp<ROR4>(r);
      r = fmaf(wt, tvf, r);
      if ((lane & 12) == 0) __builtin_amdgcn_raw_buffer_store_b16((uint16_t)cvtpk(r, r), ro, a_out, 0, AUX_NT);
    }
    return;
  }

  const size_t key0 = (size_t)grp * CTX + split * KEYS_WG + wave * KEYS_W;
  const auto rk = __builtin_amdgcn_make_buffer_rsrc((void*)(k + key0 * HD), 0, KEYS_W * HD * 2, 0x00020000);
  u32x4 kf[16][2], qf[2];
  const u32 koff = (16 * ((c >> 1) & 1) + (c >> 2)) * 256 + g * 32 + (c & 1) * 16;
#define LOADK(t) do { kf[t][0] = __builtin_amdgcn_raw_buffer_load_b128(rk, koff + ((t) & 3) * 1024, ((t) >> 2) * 8192, AUX_NT); \
                      kf[t][1] = __builtin_amdgcn_raw_buffer_load_b128(rk, koff + ((t) & 3) * 1024 + 128, ((t) >> 2) * 8192, AUX_NT); } while (0)
  LOADK(0);
  SB();
  const auto rq = __builtin_amdgcn_make_buffer_rsrc((void*)(q + grp * 8 * HD), 0, 8 * HD * 2, 0x00020000);
  const auto rv = __builtin_amdgcn_make_buffer_rsrc((void*)(v + key0 * HD), 0, KEYS_W * HD * 2, 0x00020000);
  u32x4 vf[4][8];
  const u32 voff = lane * 16;
#define LOADV(kb) _Pragma("unroll") for (int j = 0; j < 8; ++j) vf[kb][j] = __builtin_amdgcn_raw_buffer_load_b128(rv, voff + (j & 3) * 1024, (kb) * 8192 + (j >> 2) * 4096, AUX_NT)
  qf[0] = __builtin_amdgcn_raw_buffer_load_b128(rq, (c >> 1) * 256 + g * 32 + (c & 1) * 16, 0, 0);
  qf[1] = __builtin_amdgcn_raw_buffer_load_b128(rq, (c >> 1) * 256 + g * 32 + (c & 1) * 16 + 128, 0, 0);
  const u32 e_lo = __builtin_amdgcn_raw_buffer_load_b32(rw, WS_EPOCH + grp * 256, 0, AUX_SC1);
#pragma unroll
  for (int t = 1; t < 16; ++t) LOADK(t);
  LOADV(0);
  SB();
  const bool par = (c & 1) != 0;
  float s[16];
  float mx = -INFINITY;
#pragma unroll
  for (int t = 0; t < 16; ++t) {
    f32x4 d = mfma(kf[t][0], qf[0], f32x4{0.f, 0.f, 0.f, 0.f});
    d = mfma(kf[t][1], qf[1], d);
    const float ev = d[0] + dpp<XOR1>(d[1]), od = d[3] + dpp<XOR1>(d[2]);
    s[t] = par ? od : ev;
    mx = fmaxf(mx, s[t]);
  }
  mx = fmaxf(mx, dpp<XOR1>(mx));
  const float mc = x32_max(x16_max(mx)) * C2;
  if (lane < 16 && !par) smax[(lane >> 1) * 4 + wave] = mc;
  SB(); LOADV(1); LOADV(2); LOADV(3); SB();
  float ls = 0.f;
  u32x4 pf[4];
#pragma unroll
  for (int kb = 0; kb < 4; ++kb) {
    float p[4];
#pragma unroll
    for (int i = 0; i < 4; ++i) { p[i] = __builtin_amdgcn_exp2f(fmaf(s[4 * kb + i], C2, -mc)); ls += p[i]; }
    const u32 p01 = cvtpk(p[0], p[1]), p23 = cvtpk(p[2], p[3]);
    pf[kb][0] = __builtin_amdgcn_mov_dpp(p01, 0xA0, 0xf, 0xf, true); pf[kb][1] = __builtin_amdgcn_mov_dpp(p23, 0xA0, 0xf, 0xf, true);
    pf[kb][2] = __builtin_amdgcn_mov_dpp(p01, 0xF5, 0xf, 0xf, true); pf[kb][3] = __builtin_amdgcn_mov_dpp(p23, 0xF5, 0xf, 0xf, true);
  }
  ls += dpp<XOR1>(ls); ls = x32_sum(x16_sum(ls));
  if (lane < 16 && !par) lsum[(lane >> 1) * 4 + wave] = ls;
  u32 a_ow = ((wave * 8 + (c >> 1)) * OSTR + 32 * g + 16 * (c & 1)) * 4;
  const int rh = 2 * wave + (lane >> 5), d4 = lane & 31;
  u32 a_or = (rh * OSTR + 4 * d4) * 4, a_f = (L_F + 4 * rh) * 4;
  u32 a_pub = WS_O + ((grp * NSPLIT + rh * 4 + (d4 >> 3)) * NSPLIT + split) * 128 + (d4 & 7) * 16;
  u32 tag20 = (e_lo + 1u) & 0xFFFFFu;
  asm volatile("" : "+v"(a_ow), "+v"(a_or), "+v"(a_pub), "+v"(a_f), "+v"(tag20));
  SB();
  f32x4 acc[8];
#pragma unroll
  for (int e = 0; e < 8; ++e) acc[e] = f32x4{0.f, 0.f, 0.f, 0.f};
#define PV(kb) _Pragma("unroll") for (int e = 0; e < 8; ++e) { const u32 sel = (e & 1) ? 0x07060302u : 0x05040100u; u32x4 a; \
    _Pragma("unroll") for (int jj = 0; jj < 4; ++jj) a[jj] = __builtin_amdgcn_perm(vf[kb][2 * jj + 1][e >> 1], vf[kb][2 * jj][e >> 1], sel); \
    acc[e] = mfma(a, pf[kb], acc[e]); }
  PV(0); SB(); PV(1); SB(); PV(2); SB();
  {
    u32x4 a[8];
#pragma unroll
    for (int jj = 0; jj < 4; ++jj) {
      if (jj == 3) { SB(); asm volatile("s_waitcnt lgkmcnt(0)" ::: "memory"); __builtin_amdgcn_s_barrier(); SB(); }
#pragma unroll
      for (int e = 0; e < 8; ++e) a[e][jj] = __builtin_amdgcn_perm(vf[3][2 * jj + 1][e >> 1], vf[3][2 * jj][e >> 1], (e & 1) ? 0x07060302u : 0x05040100u);
      SB();
    }
#pragma unroll
    for (int e = 0; e < 8; ++e) acc[e] = mfma(a[e], pf[3], acc[e]);
  }
#pragma unroll
  for (int rr = 0; rr < 2; ++rr)
#pragma unroll
    for (int eh = 0; eh < 2; ++eh) { f32x4 t;
#pragma unroll
      for (int e = 0; e < 4; ++e) { float lo = acc[eh * 4 + e][rr], hi = acc[eh * 4 + e][2 + rr]; asm("" : "+v"(lo)); t[e] = par ? hi : lo; }
      *(f32x4*)((char*)lds + a_ow + rr * 32 + eh * 16) = t; }
  __syncthreads();
  const f32x4 f = *(const f32x4*)((char*)lds + a_f);
  const f32x4 p0 = *(const f32x4*)((char*)lds + a_or), p1 = *(const f32x4*)((char*)lds + a_or + 8 * OSTR * 4), p2 = *(const f32x4*)((char*)lds + a_or + 16 * OSTR * 4),
              p3 = *(const f32x4*)((char*)lds + a_or + 24 * OSTR * 4);
  float o[4];
#pragma unroll
  for (int i = 0; i < 4; ++i) o[i] = fma1(f[3], p3[i], fma1(f[2], p2[i], fma1(f[1], p1[i], mul1(f[0], p0[i]))));
  u32x4 item;
  { u32 lo, hi; pack_granule(o[0], o[1], tag20, lo, hi); item[0] = lo; item[1] = hi; pack_granule(o[2], o[3], tag20, lo, hi); item[2] = lo; item[3] = hi; }
  __builtin_amdgcn_raw_buffer_store_b128(item, rw, a_pub, 0, AUX_SC1);
}
}

extern "C" void kernel_launch(void* const* d_in, const int* in_sizes, int n_in, void* d_out, int out_size, void* d_ws, size_t ws_size, hipStream_t stream) {
  (void)in_sizes; (void)n_in; (void)out_size;
  const size_t ws_off = ws_size >= (size_t)WS_BYTES + (1u << 20) ? (ws_size - WS_BYTES) & ~(size_t)0xFFFFF : 0;
  tree_attn_decode<<<dim3(NGRP * NSPLIT), dim3(512), 0, stream>>>((const uint16_t*)d_in[0], (const uint16_t*)d_in[1], (const uint16_t*)d_in[2], (const uint16_t*)d_in[3],
                                                                  (const uint16_t*)d_in[4], (const uint16_t*)d_in[5], (uint16_t*)d_out, (unsigned char*)d_ws + ws_off);
}
```
